```python
import torch

def _softmax(x, axis=-1):
    return torch.softmax(x, dim=axis)
def _gelu(x):
    return torch.nn.functional.gelu(x, approximate="tanh")
def _silu(x):
    return torch.nn.functional.silu(x)
def _sigmoid(x):
    return torch.sigmoid(x)
def _layernorm(x, axis=-1, eps=1e-5):
    m = x.mean(dim=axis, keepdim=True)
    v = x.var(dim=axis, keepdim=True, unbiased=False)
    return (x - m) / torch.sqrt(v + eps)
def _scatter_add(tgt, idx, src, axis=0):
    return tgt.clone().index_add_(axis, idx, src)
def _segment_sum(src, seg, n):
    out = torch.zeros((n,) + src.shape[1:], dtype=src.dtype)
    return out.index_add_(0, seg, src)
def _pad(x, pw):
    flat = tuple(v for pair in reversed(pw) for v in pair)
    return torch.nn.functional.pad(x, flat)

kernel_name = 'concat_heads__sliding_local_dilated__linear_attn_block__prompt'

def setup_inputs():

    _w = (lambda a: (lambda t: t.to(torch.bfloat16) if t.is_floating_point() else t)(torch.as_tensor(a)))
    def _A_setup():
        B, S, D = 1, 2048, 640
        return {
            "x": _w((lambda *s: torch.randn(*s, dtype=torch.bfloat16) * 0.2)(B, S, D)),
            "wqkv": _w((lambda *s: torch.randn(*s, dtype=torch.bfloat16) * 0.2)(D, 3 * D)),
            "wo": _w((lambda *s: torch.randn(*s, dtype=torch.bfloat16) * 0.05)(D, D)),
        }
    _A_in = _A_setup()

    def _B_setup():
        B, S, D = 1, 2048, 640
        return {
            "x": _w((lambda *s: torch.randn(*s, dtype=torch.bfloat16) * 0.05)(B, S, D)),
            "wqkv": _w((lambda *s: torch.randn(*s, dtype=torch.bfloat16) * 1.6)(D, 3 * D)),
            "wo": _w((lambda *s: torch.randn(*s, dtype=torch.bfloat16) * 0.8)(D, D)),
        }
    _B_in = _B_setup()

    return {
        "A_x": _A_in["x"],
        "A_wqkv": _A_in["wqkv"],
        "A_wo": _A_in["wo"],
        "B_x": _B_in["x"],
        "B_wqkv": _B_in["wqkv"],
        "B_wo": _B_in["wo"],
    }

def reference(A_x, A_wqkv, A_wo, B_x, B_wqkv, B_wo):
    def _A_ref(x, wqkv, wo):
        B, S, D = x.shape
        H = 32; Dh = D // H
        W, dil = 8, 2
        qkv = (lambda x, *d: x.permute(*d))((x @ wqkv).reshape(B, S, 3, H, Dh), 2, 0, 3, 1, 4)
        q, k, v = qkv[0], qkv[1], qkv[2]
        scale = Dh ** -0.5
        pos = torch.arange(S)
        off = torch.arange(W) * dil
        idx = pos[:, None] - off[None, :]
        mask = idx >= 0
        idx_c = torch.where(mask, idx, 0).reshape(-1)
        kg = k[:, :, idx_c, :].reshape(B, H, S, W, Dh)
        vg = v[:, :, idx_c, :].reshape(B, H, S, W, Dh)
        logits = torch.einsum("bhsd,bhswd->bhsw", q, kg) * scale
        logits = torch.where(mask[None, None, :, :], logits, -1e9)
        attn = _softmax(logits, axis=-1)
        o = torch.einsum("bhsw,bhswd->bhsd", attn, vg)
        return (lambda x, *d: x.permute(*d))(o, 0, 2, 1, 3).reshape(B, S, D) @ wo

    def _B_ref(x, wqkv, wo):
        B, S, D = x.shape
        H = 32; Dh = D // H
        qkv = (lambda x, *d: x.permute(*d))((x @ wqkv).reshape(B, S, 3, H, Dh), 2, 0, 3, 1, 4)
        q, k, v = qkv[0], qkv[1], qkv[2]
        phi = lambda t: torch.where(t > 0, t + 1, torch.exp(t)) + 1e-6
        pq, pk = phi(q), phi(k)
        kv = torch.einsum("bhsd,bhse->bhde", pk, v)
        z = torch.einsum("bhsd->bhd", pk)
        num = torch.einsum("bhqd,bhde->bhqe", pq, kv)
        den = torch.einsum("bhqd,bhd->bhq", pq, z)[..., None] + 1e-6
        o = num / den
        return (lambda x, *d: x.permute(*d))(o, 0, 2, 1, 3).reshape(B, S, D) @ wo

    a = _A_ref(A_x, A_wqkv, A_wo)
    b = _B_ref(B_x, B_wqkv, B_wo)
    return torch.concatenate([a, b], axis=-1)
```

```cpp
#include <hip/hip_runtime.h>
#include <hip/hip_bf16.h>
typedef __hip_bfloat16 bf16;
constexpr int S=2048, D=640, H=32, DH=20;

__device__ __forceinline__ float bf2f(bf16 v){ return __bfloat162float(v); }
__device__ __forceinline__ bf16 f2bf(float v){ return __float2bfloat16(v); }
__device__ __forceinline__ float rbf(float v){ return bf2f(f2bf(v)); }

__global__ void gemm_naive(const bf16* A, const bf16* B, bf16* C, int M, int N, int K, int ldc){
  __shared__ float As[16][17], Bs[16][17];
  int tx=threadIdx.x&15, ty=threadIdx.x>>4;
  int row=blockIdx.y*16+ty, col=blockIdx.x*16+tx;
  float acc=0;
  for(int k0=0;k0<K;k0+=16){
    As[ty][tx]=bf2f(A[row*K+k0+tx]);
    Bs[ty][tx]=bf2f(B[(k0+ty)*N+col]);
    __syncthreads();
    for(int k=0;k<16;k++) acc+=As[ty][k]*Bs[k][tx];
    __syncthreads();
  }
  C[row*ldc+col]=f2bf(acc);
}

__global__ void attnA(const bf16* qkv, bf16* o){
  int idx=blockIdx.x*blockDim.x+threadIdx.x;
  if(idx>=S*H) return;
  int s=idx/H, h=idx%H;
  const float scale=rbf(0.22360679774997896f);
  float q[DH];
  for(int d=0;d<DH;d++) q[d]=bf2f(qkv[s*3*D+h*DH+d]);
  float lg[8]; float m=-INFINITY;
  for(int w=0;w<8;w++){
    int j=s-2*w;
    float v;
    if(j>=0){
      float a=0;
      for(int d=0;d<DH;d++) a+=q[d]*bf2f(qkv[j*3*D+D+h*DH+d]);
      v=rbf(rbf(a)*0.22360679774997896f);
    } else v=rbf(-1e9f);
    lg[w]=v; m=fmaxf(m,v);
  }
  float sum=0;
  for(int w=0;w<8;w++){ lg[w]=__expf(lg[w]-m); sum+=lg[w]; }
  float acc[DH];
  for(int d=0;d<DH;d++) acc[d]=0;
  for(int w=0;w<8;w++){
    int j=s-2*w; if(j<0) j=0;
    float p=rbf(lg[w]/sum);
    for(int d=0;d<DH;d++) acc[d]+=p*bf2f(qkv[j*3*D+2*D+h*DH+d]);
  }
  for(int d=0;d<DH;d++) o[s*D+h*DH+d]=f2bf(acc[d]);
}

__device__ __forceinline__ float phi(float t){
  float r = t>0.f ? rbf(t+1.f) : rbf(__expf(t));
  return rbf(r+1e-6f);
}

__global__ void kvB(const bf16* qkv, float* kv){
  int h=blockIdx.x, t=threadIdx.x;
  if(t>=420) return;
  int d=t/21, e=t%21;
  float acc=0;
  for(int s=0;s<S;s++){
    float pk=phi(bf2f(qkv[s*3*D+D+h*DH+d]));
    float v= e<20? bf2f(qkv[s*3*D+2*D+h*DH+e]) : 1.f;
    acc+=pk*v;
  }
  kv[h*420+t]=rbf(acc);
}

__global__ void oB(const bf16* qkv, const float* kv, bf16* o){
  int idx=blockIdx.x*blockDim.x+threadIdx.x;
  if(idx>=S*H) return;
  int s=idx/H, h=idx%H;
  float pq[DH];
  for(int d=0;d<DH;d++) pq[d]=phi(bf2f(qkv[s*3*D+h*DH+d]));
  float den=0;
  for(int d=0;d<DH;d++) den+=pq[d]*kv[h*420+d*21+20];
  den=rbf(rbf(den)+1e-6f);
  for(int e=0;e<DH;e++){
    float num=0;
    for(int d=0;d<DH;d++) num+=pq[d]*kv[h*420+d*21+e];
    o[s*D+h*DH+e]=f2bf(rbf(num)/den);
  }
}

extern "C" void kernel_launch(void* const* d_in, const int* in_sizes, int n_in,
                              void* d_out, int out_size, void* d_ws, size_t ws_size,
                              hipStream_t stream) {
  const bf16* Ax=(const bf16*)d_in[0]; const bf16* Aw=(const bf16*)d_in[1]; const bf16* Ao=(const bf16*)d_in[2];
  const bf16* Bx=(const bf16*)d_in[3]; const bf16* Bw=(const bf16*)d_in[4]; const bf16* Bo=(const bf16*)d_in[5];
  bf16* out=(bf16*)d_out;
  char* ws=(char*)d_ws;
  bf16* qkvA=(bf16*)ws; bf16* qkvB=qkvA+S*3*D; bf16* oA=qkvB+S*3*D; bf16* oBp=oA+S*D; float* kv=(float*)(oBp+S*D);
  gemm_naive<<<dim3(3*D/16,S/16),256,0,stream>>>(Ax,Aw,qkvA,S,3*D,D,3*D);
  gemm_naive<<<dim3(3*D/16,S/16),256,0,stream>>>(Bx,Bw,qkvB,S,3*D,D,3*D);
  attnA<<<S*H/256,256,0,stream>>>(qkvA,oA);
  kvB<<<H,448,0,stream>>>(qkvB,kv);
  oB<<<S*H/256,256,0,stream>>>(qkvB,kv,oBp);
  gemm_naive<<<dim3(D/16,S/16),256,0,stream>>>(oA,Ao,out,S,D,D,2*D);
  gemm_naive<<<dim3(D/16,S/16),256,0,stream>>>(oBp,Bo,out+D,S,D,D,2*D);
}
```

```cpp
#include <hip/hip_runtime.h>
#include <hip/hip_bf16.h>

typedef __bf16 bf8 __attribute__((ext_vector_type(8)));
typedef __bf16 bf4 __attribute__((ext_vector_type(4)));
typedef __bf16 bf2 __attribute__((ext_vector_type(2)));
typedef float f4 __attribute__((ext_vector_type(4)));
typedef float f2 __attribute__((ext_vector_type(2)));
typedef unsigned u4 __attribute__((ext_vector_type(4)));
typedef unsigned u2 __attribute__((ext_vector_type(2)));
typedef unsigned long long u64;
#define GAS __attribute__((address_space(1)))
#define LAS __attribute__((address_space(3)))
#define DEV __device__ __forceinline__

template <int V> struct IC { static constexpr int value = V; };
#define SPLIT 0

constexpr unsigned WS_EPOCH = 0;
constexpr unsigned WS_FKV = 4096;
constexpr unsigned WS_FO = WS_FKV + 128 * 128;
constexpr unsigned WS_KVP = 65536;
constexpr unsigned KVP_STRIDE = 7168;
constexpr unsigned WS_O = 1u << 20;
constexpr unsigned WS_STAMP = 8u << 20;
constexpr unsigned WS_TOTAL = 9u << 20;

constexpr unsigned XB = 144 * 128, WB = 64 * 480, STB = XB + WB;
constexpr unsigned WO_OFF = 0;
constexpr unsigned EX_OFF = 102400;
constexpr unsigned LDS_TOTAL = 163840;
constexpr unsigned TAG_OFF = LDS_TOTAL - 16;

DEV void glds16(const char* base, unsigned off, unsigned lds) {
    asm volatile("s_mov_b32 m0, %2\n\ts_nop 0\n\tglobal_load_lds_dwordx4 %0, %1" ::"v"(off), "s"(base), "s"(lds) : "memory");
}
#define SB __builtin_amdgcn_sched_barrier(0)
template <int N> DEV void wait_vm() { asm volatile("s_waitcnt vmcnt(%0)" ::"i"(N) : "memory"); }
DEV void bar() { asm volatile("s_waitcnt lgkmcnt(0)\n\ts_barrier" ::: "memory"); }
DEV bf8 lds128(unsigned a) { return *(LAS bf8*)(size_t)a; }
DEV u2 lds64(unsigned a) { return *(LAS u2*)(size_t)a; }
DEV void sts64(unsigned a, u2 v) { *(LAS u2*)(size_t)a = v; }
DEV void sts128(unsigned a, u4 v) { *(LAS u4*)(size_t)a = v; }
DEV bf4 ldtr(unsigned a) { return __builtin_amdgcn_ds_read_tr16_b64_v4bf16((LAS bf4*)(size_t)a); }
DEV bf8 cat(bf4 a, bf4 b) { return __builtin_shufflevector(a, b, 0, 1, 2, 3, 4, 5, 6, 7); }
DEV bf8 cat(u2 a, u2 b) { u4 r = {a.x, a.y, b.x, b.y}; return __builtin_bit_cast(bf8, r); }
DEV f4 mfma(bf8 a, bf8 b, f4 c) { return __builtin_amdgcn_mfma_f32_16x16x32_bf16(a, b, c, 0, 0, 0); }
DEV unsigned pk2(float a, float b) { f2 t = {a, b}; return __builtin_bit_cast(unsigned, __builtin_convertvector(t, bf2)); }
DEV u2 pk4(f4 v) { u2 r = {pk2(v[0], v[1]), pk2(v[2], v[3])}; return r; }
DEV float phi(float t) {
    float e = __builtin_amdgcn_exp2f(fminf(t * 1.4426950408889634f, 0.f));
    return (fmaxf(t, 0.f) + e) + 1e-6f;
}
DEV f4 phi4(f4 v) { f4 r = {phi(v[0]), phi(v[1]), phi(v[2]), phi(v[3])}; return r; }
DEV u64 mix64(u64 z) {
    z += 0x9E3779B97F4A7C15ull;
    z = (z ^ (z >> 30)) * 0xBF58476D1CE4E5B9ull;
    z = (z ^ (z >> 27)) * 0x94D049BB133111EBull;
    return z ^ (z >> 31);
}
DEV u64 ld_agent(const char* p) { return __hip_atomic_load((GAS u64*)p, __ATOMIC_RELAXED, __HIP_MEMORY_SCOPE_AGENT); }
DEV void st_agent(char* p, u64 v) { __hip_atomic_store((GAS u64*)p, v, __ATOMIC_RELAXED, __HIP_MEMORY_SCOPE_AGENT); }
DEV void poll_all(const char* p, u64 tag) {
#pragma unroll 1
    for (unsigned spins = 0; spins < (1u << 18); ++spins) {
        u64 v = ld_agent(p);
        if (__all(v == tag)) break;
        __builtin_amdgcn_s_sleep(1);
    }
    __builtin_amdgcn_fence(__ATOMIC_ACQUIRE, "wavefront");
}
DEV unsigned fb(float x) { return __builtin_bit_cast(unsigned, x); }
DEV float bf(unsigned x) { return __builtin_bit_cast(float, x); }
DEV unsigned opaque(unsigned x) { asm volatile("" : "+v"(x)); return x; }
DEV void rowmax4x4(float (&m)[4]) {
    auto a = __builtin_amdgcn_permlane16_swap(fb(m[0]), fb(m[1]), false, false);
    auto b = __builtin_amdgcn_permlane16_swap(fb(m[2]), fb(m[3]), false, false);
    float u = fmaxf(bf(a[0]), bf(a[1])), w = fmaxf(bf(b[0]), bf(b[1]));
    auto d = __builtin_amdgcn_permlane32_swap(fb(u), fb(w), false, false);
    unsigned t = fb(fmaxf(bf(d[0]), bf(d[1])));
    auto e = __builtin_amdgcn_permlane32_swap(t, opaque(t), false, false);
    auto f = __builtin_amdgcn_permlane16_swap(e[0], opaque(e[0]), false, false);
    auto h = __builtin_amdgcn_permlane16_swap(e[1], opaque(e[1]), false, false);
    m[0] = bf(f[0]); m[1] = bf(f[1]); m[2] = bf(h[0]); m[3] = bf(h[1]);
}

#define STAMP(i)

DEV void store_o(__amdgpu_buffer_rsrc_t rs, unsigned obase, int par, int lane, const u2 (&t)[5]) {
    u4 a = {t[0].x, t[0].y, t[1].x, t[1].y}, b = {t[2].x, t[2].y, t[3].x, t[3].y};
    __builtin_amdgcn_raw_buffer_store_b128(a, rs, obase + par * 2048 + lane * 16, 0, 16);
    __builtin_amdgcn_raw_buffer_store_b128(b, rs, obase + par * 2048 + 1024 + lane * 16, 0, 16);
    __builtin_amdgcn_raw_buffer_store_b64(t[4], rs, obase + 4096 + par * 512 + lane * 8, 0, 16);
}

DEV void attn_tile(const u2 (&q)[5], const u2 (&ko)[5], const u2 (&kp)[5], const u2 (&vo)[5], const u2 (&vp)[5],
                   bool prev_ok, int lane, u2 (&out)[5]) {
    const int c = lane & 15, g = lane >> 4;
    const float sc = 0.22360679774997896f * 1.4426950408889634f;
    const u2 zero2 = {0u, 0u};
    const u4 ones4 = {0x3F803F80u, 0x3F803F80u, 0x3F803F80u, 0x3F803F80u};
    const bf8 ones = __builtin_bit_cast(bf8, ones4);
    const f4 z4 = {0.f, 0.f, 0.f, 0.f};
    bool own[4], val[4];
#pragma unroll
    for (int r = 0; r < 4; ++r) {
        int kap = 4 * g + r;
        own[r] = kap <= c;
        val[r] = own[r] ? (kap >= c - 7) : (prev_ok && kap >= c + 9);
    }
    const unsigned mk01 = (own[0] ? 0xFFFFu : 0u) | (own[1] ? 0xFFFF0000u : 0u), mk23 = (own[2] ? 0xFFFFu : 0u) | (own[3] ? 0xFFFF0000u : 0u);
    bf8 P[4];
    float rs[4], s[4][4], m[4];
#pragma unroll
    for (int h = 0; h < 4; ++h) {
        bf8 qf = cat(g >= h ? q[h] : zero2, g <= h ? q[h + 1] : zero2);
        f4 so = mfma(cat(ko[h], ko[h + 1]), qf, z4);
        f4 sp = mfma(cat(kp[h], kp[h + 1]), qf, z4);
#pragma unroll
        for (int r = 0; r < 4; ++r) s[h][r] = val[r] ? (own[r] ? so[r] : sp[r]) : -INFINITY;
        m[h] = fmaxf(fmaxf(s[h][0], s[h][1]), fmaxf(s[h][2], s[h][3]));
    }
    rowmax4x4(m);
#pragma unroll
    for (int h = 0; h < 4; ++h) {
        float p[4];
        const float ms = m[h] * sc;
#pragma unroll
        for (int r = 0; r < 4; ++r) p[r] = __builtin_amdgcn_exp2f(__builtin_fmaf(s[h][r], sc, -ms));
        const unsigned p01 = pk2(p[0], p[1]), p23 = pk2(p[2], p[3]);
        u2 pp = {p01 & ~mk01, p23 & ~mk23};
        u2 po = {p01 & mk01, p23 & mk23};
        P[h] = cat(pp, po);
        f4 sm = mfma(ones, P[h], z4);
        rs[h] = __builtin_amdgcn_rcpf(sm[0]);
    }
#pragma unroll
    for (int i = 0; i < 5; ++i) {
        bf8 vf = cat(vp[i], vo[i]);
        f4 o;
        float r;
        if (i == 0) { o = mfma(vf, P[0], z4); r = rs[0]; }
        else if (i == 4) { o = mfma(vf, P[3], z4); r = rs[3]; }
        else {
            f4 a = mfma(vf, P[i - 1], z4), b = mfma(vf, P[i], z4);
            bool lo = g < i;
            o = lo ? a : b;
            r = lo ? rs[i - 1] : rs[i];
        }
        out[i] = pk4(o * r);
    }
}

template <int PH>
__global__ __launch_bounds__(512, 2) void fused_kernel(const char* __restrict__ xA, const char* __restrict__ wA, const char* __restrict__ woA,
                                                       const char* __restrict__ xB, const char* __restrict__ wB, const char* __restrict__ woB,
                                                       char* __restrict__ out, char* __restrict__ ws) {
    extern __shared__ __attribute__((aligned(16))) char smem[];
    const int bid = blockIdx.x, xcd = bid & 7, j = bid >> 3;
    const int br = xcd >> 2, xq = xcd & 3;
    const int rt = (xq >> 1) * 8 + (j & 7), hg = (xq & 1) * 4 + (j >> 3);
    const int rt2 = xq * 4 + (j & 3), ct = j >> 2;
    const char* x = br ? xB : xA;
    const char* w = br ? wB : wA;
    const char* wo = br ? woB : woA;
    const int tid = threadIdx.x, lane = tid & 63;
    const int wv = __builtin_amdgcn_readfirstlane(tid >> 6);
    const int c = lane & 15, g = lane >> 4;
    const __amdgpu_buffer_rsrc_t rs = __builtin_amdgcn_make_buffer_rsrc(ws, 0, WS_TOTAL, 0x00020000);
    const f4 z4 = {0.f, 0.f, 0.f, 0.f};
    STAMP(0);
    u64 tag = 0;

    const unsigned wlane = XB + (16 * (g >> 1) + 4 * (g & 1) + ((lane >> 2) & 3)) * 480 + (lane & 3) * 8;

    if (wv < 4) {
        const int cw = wv;
        if constexpr (PH & 1) {
            tag = mix64(ld_agent(ws + WS_EPOCH));
            if (tid == 0) *(LAS u64*)(size_t)TAG_OFF = tag;
            f4 acc[2][15];
#pragma unroll
            for (int m = 0; m < 2; ++m)
#pragma unroll
                for (int n = 0; n < 15; ++n) acc[m][n] = z4;
            const unsigned xl0 = (16 * (1 + 2 * cw) + c) * 128 + ((g ^ (c >> 1)) & 7) * 16;
            const unsigned xl1 = (16 * (1 + 2 * cw) + c) * 128 + (((4 + g) ^ (c >> 1)) & 7) * 16;
            auto mainloop = [&](auto BRC) {
                constexpr int BR = decltype(BRC)::value;
                bf8 wA_[5], wB_[5], xk0[2], xk1[2];
                auto chunk = [&](auto CHC, auto FRONT, auto RDX, const bf8 (&cur)[5], const bf8 (&xc)[2], bf8 (&nxt)[5], unsigned na, bf8 (&xn)[2], unsigned xa) {
                    constexpr int CH = decltype(CHC)::value;
                    constexpr bool Y = CH < (BR ? 1 : 2);
                    bf4 lo[5], hi[5];
#pragma unroll
                    for (int i = 0; i < 5; ++i) {
                        acc[0][CH * 5 + i] = Y ? mfma(cur[i], xc[0], acc[0][CH * 5 + i]) : mfma(xc[0], cur[i], acc[0][CH * 5 + i]);
                        if constexpr (decltype(FRONT)::value) { if (2 * i < 5) { lo[2 * i] = ldtr(na + 2 * i * 32); hi[2 * i] = ldtr(na + 2 * i * 32 + 3840); } }
                        else { if (decltype(RDX)::value && i == 0) xn[0] = lds128(xa); lo[i] = ldtr(na + i * 32); }
                        SB;
                        acc[1][CH * 5 + i] = Y ? mfma(cur[i], xc[1], acc[1][CH * 5 + i]) : mfma(xc[1], cur[i], acc[1][CH * 5 + i]);
                        if constexpr (decltype(FRONT)::value) { if (2 * i + 1 < 5) { lo[2 * i + 1] = ldtr(na + (2 * i + 1) * 32); hi[2 * i + 1] = ldtr(na + (2 * i + 1) * 32 + 3840); } }
                        else { hi[i] = ldtr(na + i * 32 + 3840); if (decltype(RDX)::value && i == 0) xn[1] = lds128(xa + 2048); }
                        SB;
                    }
#pragma unroll
                    for (int i = 0; i < 5; ++i) nxt[i] = cat(lo[i], hi[i]);
                };
                unsigned slot = 2 * STB;
                bar();
                xk0[0] = lds128(slot + xl0); xk0[1] = lds128(slot + xl0 + 2048);
#pragma unroll
                for (int i = 0; i < 5; ++i) wA_[i] = cat(ldtr(slot + wlane + i * 32), ldtr(slot + wlane + i * 32 + 3840));
                SB;
                for (int ks = 0; ks < 10; ++ks) {
                    const unsigned wa = slot + wlane;
                    chunk(IC<0>{}, IC<0>{}, IC<0>{}, wA_, xk0, wB_, wa + 160, xk1, 0u);
                    chunk(IC<1>{}, IC<0>{}, IC<0>{}, wB_, xk0, wA_, wa + 320, xk1, 0u);
                    chunk(IC<2>{}, IC<0>{}, IC<1>{}, wA_, xk0, wB_, wa + 15360, xk1, slot + xl1);
                    chunk(IC<0>{}, IC<0>{}, IC<0>{}, wB_, xk1, wA_, wa + 15360 + 160, xk0, 0u);
                    chunk(IC<1>{}, IC<1>{}, IC<0>{}, wA_, xk1, wB_, wa + 15360 + 320, xk0, 0u);
                    slot += STB; if (slot == 3 * STB) slot = 0;
                    bar();
                    SB;
                    chunk(IC<2>{}, IC<0>{}, IC<1>{}, wB_, xk1, wA_, slot + wlane, xk0, slot + xl0);
                }
            };
            if (br) mainloop(IC<1>{}); else mainloop(IC<0>{});
            STAMP(1);
            const unsigned obase = WS_O + (((br * 16 + rt) * 4 + cw) * 8 + hg) * 5120;
            if (br == 0) {
                u2 q[5], ko[5], vo[5];
#pragma unroll
                for (int i = 0; i < 5; ++i) {
                    q[i] = pk4(acc[0][i]); ko[i] = pk4(acc[0][5 + i]); vo[i] = pk4(acc[0][10 + i]);
                    sts64(EX_OFF + ((1 + 2 * cw) * 10 + i) * 512 + lane * 8, ko[i]);
                    sts64(EX_OFF + ((1 + 2 * cw) * 10 + 5 + i) * 512 + lane * 8, vo[i]);
                    sts64(EX_OFF + ((2 + 2 * cw) * 10 + i) * 512 + lane * 8, pk4(acc[1][5 + i]));
                    sts64(EX_OFF + ((2 + 2 * cw) * 10 + 5 + i) * 512 + lane * 8, pk4(acc[1][10 + i]));
                    sts64(EX_OFF + (90 + cw * 5 + i) * 512 + lane * 8, pk4(acc[1][i]));
                }
                bar();
                STAMP(2);
                u2 kp[5], vp[5], o[5];
                const int Tp = cw == 0 ? 0 : 2 * cw - 1;
#pragma unroll
                for (int i = 0; i < 5; ++i) {
                    kp[i] = lds64(EX_OFF + (Tp * 10 + i) * 512 + lane * 8);
                    vp[i] = lds64(EX_OFF + (Tp * 10 + 5 + i) * 512 + lane * 8);
                }
                attn_tile(q, ko, kp, vo, vp, (rt | cw) != 0, lane, o);
                store_o(rs, obase, 0, lane, o);
                STAMP(3); STAMP(4);
            } else {
#pragma unroll
                for (int i = 0; i < 5; ++i) {
                    u2 a = pk4(phi4(acc[0][5 + i])), b = pk4(phi4(acc[1][5 + i]));
                    u4 t = {a.x, a.y, b.x, b.y};
                    sts128(EX_OFF + (cw * 10 + i) * 1024 + lane * 16, t);
                    u2 a2 = pk4(acc[0][10 + i]), b2 = pk4(acc[1][10 + i]);
                    u4 t2 = {a2.x, a2.y, b2.x, b2.y};
                    sts128(EX_OFF + (cw * 10 + 5 + i) * 1024 + lane * 16, t2);
                }
                {
                    const u4 zz = {0u, 0u, 0u, 0u};
#pragma unroll
                    for (int i = 0; i < 4; ++i) sts128(EX_OFF + 40960 + i * 4096 + tid * 16, zz);
                }
                bar();
                STAMP(2);
                u2 pq[2][5];
#pragma unroll
                for (int m = 0; m < 2; ++m)
#pragma unroll
                    for (int i = 0; i < 5; ++i) pq[m][i] = pk4(phi4(acc[m][i]));
                bar();
                bar();
                STAMP(3);
                {
                    f4 s = z4;
                    if (tid < 420) {
                        u4 v[16];
#pragma unroll
                        for (int r = 0; r < 16; ++r) v[r] = __builtin_amdgcn_raw_buffer_load_b128(rs, WS_KVP + (hg * 16 + r) * KVP_STRIDE + tid * 16, 0, 16);
#pragma unroll
                        for (int r = 0; r < 16; ++r) s += __builtin_bit_cast(f4, v[r]);
                        int u = tid / 80, e = tid - u * 80, h = e / 20;
                        sts64(EX_OFF + 40960 + e * 176 + (5 * h + u) * 8, pk4(s));
                    }
                }
                bar();
                STAMP(4);
                const unsigned ia = EX_OFF + 40960 + c * 176 + g * 8;
                const unsigned za = EX_OFF + 40960 + (80 + (c & 3)) * 176 + g * 8;
                const u2 zero2 = {0u, 0u};
                bf8 zf0 = cat(lds64(za), lds64(za + 32)), zf1 = cat(lds64(za + 64), lds64(za + 96)), zf2 = cat(lds64(za + 128), zero2);
                bf8 kf[9];
                kf[0] = cat(lds64(ia), lds64(ia + 32));
                kf[1] = cat(lds64(ia + 2816), lds64(ia + 2816 + 32));
                kf[2] = cat(lds64(ia + 2816 + 64), lds64(ia + 2816 + 96));
                kf[3] = cat(lds64(ia + 2 * 2816), lds64(ia + 2 * 2816 + 32));
                kf[4] = cat(lds64(ia + 2 * 2816 + 64), lds64(ia + 2 * 2816 + 96));
                kf[5] = cat(lds64(ia + 3 * 2816 + 64), lds64(ia + 3 * 2816 + 96));
                kf[6] = cat(lds64(ia + 3 * 2816 + 128), zero2);
                kf[7] = cat(lds64(ia + 4 * 2816 + 64), lds64(ia + 4 * 2816 + 96));
                kf[8] = cat(lds64(ia + 4 * 2816 + 128), zero2);
#pragma unroll
                for (int m = 0; m < 2; ++m) {
                    bf8 p01 = cat(pq[m][0], pq[m][1]), p23 = cat(pq[m][2], pq[m][3]), p4 = cat(pq[m][4], zero2);
                    f4 den = mfma(zf2, p4, mfma(zf1, p23, mfma(zf0, p01, z4)));
                    f4 n0 = mfma(kf[0], p01, z4);
                    f4 n1 = mfma(kf[2], p23, mfma(kf[1], p01, z4));
                    f4 n2 = mfma(kf[4], p23, mfma(kf[3], p01, z4));
                    f4 n3 = mfma(kf[6], p4, mfma(kf[5], p23, z4));
                    f4 n4 = mfma(kf[8], p4, mfma(kf[7], p23, z4));
                    float r0 = __builtin_amdgcn_rcpf(den[0] + 1e-6f), r1 = __builtin_amdgcn_rcpf(den[1] + 1e-6f);
                    float r2 = __builtin_amdgcn_rcpf(den[2] + 1e-6f), r3 = __builtin_amdgcn_rcpf(den[3] + 1e-6f);
                    u2 o[5];
                    o[0] = pk4(n0 * r0);
                    o[1] = pk4(n1 * (g < 1 ? r0 : r1));
                    o[2] = pk4(n2 * (g < 2 ? r1 : r2));
                    o[3] = pk4(n3 * (g < 3 ? r2 : r3));
                    o[4] = pk4(n4 * r3);
                    store_o(rs, obase, m, lane, o);
                }
            }
            wait_vm<0>();
            bar();
            if (tid == 0) st_agent(ws + WS_FO + ((br * 16 + rt) * 8 + hg) * 128, tag);
            STAMP(5);
        }
        if constexpr (PH & 2) {
            if constexpr (PH == 2) bar();
            const unsigned wl = WO_OFF + (lane >> 2) * 160 + (lane & 3) * 8;
            unsigned gb[9];
#pragma unroll
            for (int gg = 0; gg < 9; ++gg) gb[gg] = wl + ((gg + ct) & 7) * 12800;
            bf8 wA_[5], wB_[5];
#pragma unroll
            for (int i = 0; i < 5; ++i) wA_[i] = cat(ldtr(gb[0] + i * 32), ldtr(gb[0] + i * 32 + 16 * 160));
            if constexpr (PH == 3) poll_all(ws + WS_FO + ((br * 16 + rt2) * 8 + (lane & 7)) * 128, tag);
            STAMP(6);
            const unsigned ob = WS_O + ((br * 16 + rt2) * 4 + cw) * 8 * 5120;
            u4 of[8][4];
            u2 ot[8][2];
#pragma unroll
            for (int gg = 0; gg < 8; ++gg) {
                const unsigned so = ob + ((gg + ct) & 7) * 5120;
                of[gg][0] = __builtin_amdgcn_raw_buffer_load_b128(rs, lane * 16, so, 16);
                of[gg][2] = __builtin_amdgcn_raw_buffer_load_b128(rs, 2048 + lane * 16, so, 16);
                of[gg][1] = __builtin_amdgcn_raw_buffer_load_b128(rs, 1024 + lane * 16, so, 16);
                of[gg][3] = __builtin_amdgcn_raw_buffer_load_b128(rs, 3072 + lane * 16, so, 16);
#pragma unroll
                for (int b = 0; b < 2; ++b) ot[gg][b] = __builtin_amdgcn_raw_buffer_load_b64(rs, b * 512 + lane * 8, so + 4096, 16);
            }
            SB;
            f4 a2[2][5];
#pragma unroll
            for (int m = 0; m < 2; ++m)
#pragma unroll
                for (int n = 0; n < 5; ++n) a2[m][n] = z4;
            auto step = [&](const bf8 (&cur)[5], bf8 o0, bf8 o1, bf8 (&nxt)[5], unsigned na, unsigned nb) {
                bf4 lo[5], hi[5];
#pragma unroll
                for (int i = 0; i < 5; ++i) {
                    a2[0][i] = mfma(cur[i], o0, a2[0][i]); lo[i] = ldtr(na + i * 32); SB;
                    a2[1][i] = mfma(cur[i], o1, a2[1][i]); hi[i] = ldtr(nb + i * 32); SB;
                }
#pragma unroll
                for (int i = 0; i < 5; ++i) nxt[i] = cat(lo[i], hi[i]);
            };
#define B8(x) __builtin_bit_cast(bf8, x)
#pragma unroll
            for (int u = 0; u < 4; ++u) {
                const unsigned g0 = gb[2 * u], g1 = gb[2 * u + 1], g2 = gb[2 * u + 2];
                if (u & 1) {
                    step(wB_, B8(of[2 * u][0]), B8(of[2 * u][2]), wA_, g0 + 32 * 160, g0 + 48 * 160);
                    step(wA_, B8(of[2 * u][1]), B8(of[2 * u][3]), wB_, g1, g1 + 16 * 160);
                    step(wB_, B8(of[2 * u + 1][0]), B8(of[2 * u + 1][2]), wA_, g1 + 32 * 160, g1 + 48 * 160);
                    step(wA_, B8(of[2 * u + 1][1]), B8(of[2 * u + 1][3]), wB_, g0 + 64 * 160, g1 + 64 * 160);
                    step(wB_, cat(ot[2 * u][0], ot[2 * u + 1][0]), cat(ot[2 * u][1], ot[2 * u + 1][1]), wA_, g2, g2 + 16 * 160);
                } else {
                    step(wA_, B8(of[2 * u][0]), B8(of[2 * u][2]), wB_, g0 + 32 * 160, g0 + 48 * 160);
                    step(wB_, B8(of[2 * u][1]), B8(of[2 * u][3]), wA_, g1, g1 + 16 * 160);
                    step(wA_, B8(of[2 * u + 1][0]), B8(of[2 * u + 1][2]), wB_, g1 + 32 * 160, g1 + 48 * 160);
                    step(wB_, B8(of[2 * u + 1][1]), B8(of[2 * u + 1][3]), wA_, g0 + 64 * 160, g1 + 64 * 160);
                    step(wA_, cat(ot[2 * u][0], ot[2 * u + 1][0]), cat(ot[2 * u][1], ot[2 * u + 1][1]), wB_, g2, g2 + 16 * 160);
                }
            }
            STAMP(7);
#pragma unroll
            for (int m = 0; m < 2; ++m) {
                char* op = out + (size_t)(rt2 * 128 + 32 * cw + 2 * c + m) * 2560 + br * 1280 + ct * 160 + g * 8;
#pragma unroll
                for (int n = 0; n < 5; ++n) *(GAS u2*)(op + n * 32) = pk4(a2[m][n]);
            }
            STAMP(8);
        }
    } else {
        const int lw = wv - 4;
        auto wo_load = [&](int i0, int i1) {
            for (int i = i0; i < i1; ++i) {
                int ch = (lw + 4 * i) * 64 + lane;
                int r = (ch * 6554) >> 16;
                glds16(wo, r * (1280 - 160) + ch * 16 + ct * 160, WO_OFF + (lw + 4 * i) * 1024);
            }
        };
        if constexpr (PH & 1) {
            const int r0 = rt * 128;
            unsigned off[12];
#pragma unroll
            for (int i = 0; i < 5; ++i) {
                int rho = (lw + 4 * i) * 8 + (lane >> 3);
                int T = rho >> 4, ii = rho & 15;
                int row = T ? r0 + 32 * ((T - 1) >> 1) + 2 * ii + ((T - 1) & 1) : (ii >= 8 ? r0 - 32 + 2 * ii : r0 - 15 + 2 * ii);
                row = max(row, 0);
                off[i] = row * 1280 + (((lane & 7) ^ (rho >> 1)) & 7) * 16;
            }
#pragma unroll
            for (int i = 4; i < 12; ++i) {
                int ch = (lw + 4 * i - 18) * 64 + lane;
                int pos = (ch * 2185) >> 16, cc = ch - pos * 30;
                int k = (pos & ~12) | ((pos & 4) << 1) | ((pos & 8) >> 1);
                int sg = (cc * 26) >> 8;
                unsigned o = k * 3840 + sg * (1280 - 160) + hg * 160 + cc * 16;
                if (i == 4) { if (lw >= 2) off[i] = o; } else off[i] = o;
            }
#define KORD 1
            u64 seq;
            if (KORD == 0) seq = 0x9876543210ull;
            else if (KORD == 1) {
                int ph = ((5 * (j & 7)) >> 2) + ((5 * (j >> 3)) >> 1); if (ph >= 10) ph -= 10;
                seq = (0x9876543210ull >> (4 * ph)) | (0x9876543210ull << (4 * (10 - ph)));
            } else {
                int cl = ((j >> 3) - j) & 3;
                seq = cl == 0 ? 0x9765832140ull : cl == 1 ? 0x8769403521ull : cl == 2 ? 0x9875416032ull : 0x9865472103ull;
            }
            auto stage = [&](int t, unsigned sl) {
                const int ks = (int)(seq >> (4 * t)) & 15;
                const char* xb = x + ks * 128;
                const char* wb = w + (size_t)ks * 64 * 3840;
                unsigned lb = sl + lw * 1024;
#pragma unroll
                for (int i = 0; i < 12; ++i) { const char* b = i < 4 ? xb : (i == 4 ? (lw < 2 ? xb : wb) : wb); glds16(b, off[i], lb + i * 4096); }
            };
            stage(0, 2 * STB); stage(1, 0);
            f4 ha[3] = {z4, z4, z4};
            const int hn0 = 5 + (lw >> 1) * 5 + (lw & 1) * 3;
            const unsigned hx0 = c * 128 + ((g ^ (c >> 1)) & 7) * 16, hx1 = c * 128 + (((4 + g) ^ (c >> 1)) & 7) * 16;
            const unsigned hw = wlane + hn0 * 32, hw2 = hw + ((lw & 1) ? 32 : 64);
            unsigned slot = 2 * STB, nslot = STB;
            for (int ks = 0; ks < 10; ++ks) {
                if (ks < 9) wait_vm<12>(); else wait_vm<(PH == 3 ? 12 : 0)>();
                bar();
                if (br == 0) {
#pragma unroll
                    for (int kk = 0; kk < 2; ++kk) {
                        bf8 xf = lds128(slot + (kk ? hx1 : hx0));
                        const unsigned wa = slot + hw + kk * 32 * 480, wa2 = slot + hw2 + kk * 32 * 480;
                        bf8 w0 = cat(ldtr(wa), ldtr(wa + 8 * 480)), w1 = cat(ldtr(wa + 32), ldtr(wa + 32 + 8 * 480)), w2 = cat(ldtr(wa2), ldtr(wa2 + 8 * 480));
                        if (lw < 2) { ha[0] = mfma(w0, xf, ha[0]); ha[1] = mfma(w1, xf, ha[1]); ha[2] = mfma(w2, xf, ha[2]); }
                        else { ha[0] = mfma(xf, w0, ha[0]); ha[1] = mfma(xf, w1, ha[1]); ha[2] = mfma(xf, w2, ha[2]); }
                    }
                }
                if (ks < 8) stage(ks + 2, nslot);
                else if constexpr (PH == 3) wo_load((ks - 8) * 12, (ks - 8) * 12 + 12);
                slot += STB; if (slot == 3 * STB) slot = 0;
                nslot += STB; if (nslot == 3 * STB) nslot = 0;
            }
            bar();
            if constexpr (PH == 3) wo_load(24, 25);
            tag = *(LAS u64*)(size_t)TAG_OFF;
            const unsigned obase = WS_O + (((br * 16 + rt) * 4 + lw) * 8 + hg) * 5120;
            if (br == 0) {
                const int ht = hn0 - 5;
                sts64(EX_OFF + ht * 512 + lane * 8, pk4(ha[0]));
                sts64(EX_OFF + (ht + 1) * 512 + lane * 8, pk4(ha[1]));
                if (!(lw & 1)) sts64(EX_OFF + (ht + 2) * 512 + lane * 8, pk4(ha[2]));
                bar();
                u2 q[5], ko[5], vo[5], kp[5], vp[5], o[5];
                const int To = 2 + 2 * lw, Tp = 2 * lw;
                const int lk = lw == 0 ? lane ^ 8 : lane, lv = lw == 0 ? lane ^ 32 : lane;
#pragma unroll
                for (int i = 0; i < 5; ++i) {
                    q[i] = lds64(EX_OFF + (90 + lw * 5 + i) * 512 + lane * 8);
                    ko[i] = lds64(EX_OFF + (To * 10 + i) * 512 + lane * 8);
                    vo[i] = lds64(EX_OFF + (To * 10 + 5 + i) * 512 + lane * 8);
                    kp[i] = lds64(EX_OFF + (Tp * 10 + i) * 512 + lk * 8);
                    vp[i] = lds64(EX_OFF + (Tp * 10 + 5 + i) * 512 + lv * 8);
                }
                attn_tile(q, ko, kp, vo, vp, (rt | lw) != 0, lane, o);
                store_o(rs, obase, 1, lane, o);
            } else {
                bar();
                const u4 ones4 = {0x3F803F80u, 0x3F803F80u, 0x3F803F80u, 0x3F803F80u};
                const bf8 ones = __builtin_bit_cast(bf8, ones4);
                const unsigned pbase = WS_KVP + (hg * 16 + rt) * KVP_STRIDE;
                for (int dt = lw; dt < 5; dt += 4) {
                    f4 t[3] = {z4, z4, z4}, tz = z4;
                    const int e0 = max(dt - 1, 0), e2 = min(dt + 1, 4);
#pragma unroll
                    for (int s = 0; s < 4; ++s) {
                        bf8 kf = lds128(EX_OFF + (s * 10 + dt) * 1024 + lane * 16);
                        t[0] = mfma(kf, lds128(EX_OFF + (s * 10 + 5 + e0) * 1024 + lane * 16), t[0]);
                        t[1] = mfma(kf, lds128(EX_OFF + (s * 10 + 5 + dt) * 1024 + lane * 16), t[1]);
                        t[2] = mfma(kf, lds128(EX_OFF + (s * 10 + 5 + e2) * 1024 + lane * 16), t[2]);
                        tz = mfma(kf, ones, tz);
                    }
                    const int Q = 4 * dt + g;
#pragma unroll
                    for (int a = 0; a < 3; ++a) {
                        int et = dt - 1 + a;
                        int e = 16 * et + c, h = (e * 205) >> 12, u = Q - 5 * h;
                        if (et >= 0 && et <= 4 && u >= 0 && u <= 4)
                            __builtin_amdgcn_raw_buffer_store_b128(__builtin_bit_cast(u4, t[a]), rs, pbase + (u * 80 + e) * 16, 0, 16);
                    }
                    if (c == 0) __builtin_amdgcn_raw_buffer_store_b128(__builtin_bit_cast(u4, tz), rs, pbase + (400 + Q) * 16, 0, 16);
                }
                wait_vm<0>();
                bar();
                if (lw == 0) {
                    if (lane == 0) st_agent(ws + WS_FKV + (hg * 16 + rt) * 128, tag);
                    poll_all(ws + WS_FKV + (hg * 16 + c) * 128, tag);
                }
                bar();
                {
                    f4 s = z4;
                    if (tid < 420) {
                        u4 v[16];
#pragma unroll
                        for (int r = 0; r < 16; ++r) v[r] = __builtin_amdgcn_raw_buffer_load_b128(rs, WS_KVP + (hg * 16 + r) * KVP_STRIDE + tid * 16, 0, 16);
#pragma unroll
                        for (int r = 0; r < 16; ++r) s += __builtin_bit_cast(f4, v[r]);
                        int u = tid / 80, e = tid - u * 80, h = e / 20;
                        unsigned a = tid < 400 ? e * 176 + (5 * h + u) * 8 : (80 + (tid - 400) / 5) * 176 + (tid - 400) * 8;
                        sts64(EX_OFF + 40960 + a, pk4(s));
                    }
                }
                bar();
            }
            wait_vm<0>();
            bar();
        }
        if constexpr (PH == 2) {
            wo_load(0, 25);
            wait_vm<0>();
            bar();
        }
        if constexpr (PH & 1) {
            if (bid == 0 && lw == 3) {
#pragma unroll 1
                for (int i = 0; i < 4; ++i) poll_all(ws + WS_FO + (i * 64 + lane) * 128, tag);
                if (lane == 0) st_agent(ws + WS_EPOCH, tag);
            }
        }
    }
}

extern "C" void kernel_launch(void* const* d_in, const int* in_sizes, int n_in, void* d_out, int out_size, void* d_ws, size_t ws_size,
                              hipStream_t stream) {
    const char *xA = (const char*)d_in[0], *wA = (const char*)d_in[1], *woA = (const char*)d_in[2];
    const char *xB = (const char*)d_in[3], *wB = (const char*)d_in[4], *woB = (const char*)d_in[5];
    (void)hipFuncSetAttribute((const void*)fused_kernel<3>, hipFuncAttributeMaxDynamicSharedMemorySize, LDS_TOTAL);
    fused_kernel<3><<<256, 512, LDS_TOTAL, stream>>>(xA, wA, woA, xB, wB, woB, (char*)d_out, (char*)d_ws);
}
```

```cpp
#include <hip/hip_runtime.h>
#include <hip/hip_bf16.h>

typedef __bf16 bf8 __attribute__((ext_vector_type(8)));
typedef __bf16 bf4 __attribute__((ext_vector_type(4)));
typedef __bf16 bf2 __attribute__((ext_vector_type(2)));
typedef float f4 __attribute__((ext_vector_type(4)));
typedef float f2 __attribute__((ext_vector_type(2)));
typedef unsigned u4 __attribute__((ext_vector_type(4)));
typedef unsigned u2 __attribute__((ext_vector_type(2)));
typedef unsigned long long u64;
#define GAS __attribute__((address_space(1)))
#define LAS __attribute__((address_space(3)))
#define DEV __device__ __forceinline__
template <int V> struct IC { static constexpr int value = V; };

constexpr unsigned WS_EPOCH = 0;
constexpr unsigned WS_FKV = 4096;
constexpr unsigned WS_FO = WS_FKV + 128 * 128;
constexpr unsigned WS_KVP = 65536;
constexpr unsigned KVP_STRIDE = 7168;
constexpr unsigned WS_O = 1u << 20;
constexpr unsigned WS_STAMP = 8u << 20;
constexpr unsigned WS_TOTAL = 9u << 20;

constexpr unsigned XB = 144 * 128, WB = 64 * 480, STB = XB + WB;
constexpr unsigned WO_OFF = 0;
constexpr unsigned EX_OFF = 102400;
constexpr unsigned LDS_TOTAL = 163840;
constexpr unsigned TAG_OFF = LDS_TOTAL - 16;
constexpr unsigned IMG_OFF = EX_OFF + 40960;
constexpr unsigned DUMMY_OFF = 161792;
constexpr unsigned PQ_OFF = EX_OFF;

DEV void glds16(const char* base, unsigned off, unsigned lds) {
    asm volatile("s_mov_b32 m0, %2\n\ts_nop 0\n\tglobal_load_lds_dwordx4 %0, %1" ::"v"(off), "s"(base), "s"(lds) : "memory");
}
DEV void glds16m(const char* base, unsigned off, unsigned lds, u64 mask) {
    asm volatile("s_mov_b32 m0, %2\n\ts_mov_b64 exec, %3\n\tglobal_load_lds_dwordx4 %0, %1\n\ts_mov_b64 exec, -1" ::"v"(off), "s"(base), "s"(lds), "s"(mask) : "memory");
}
DEV void touch4m(const char* base, unsigned off, unsigned lds, u64 mask) {
    asm volatile("s_mov_b32 m0, %2\n\ts_mov_b64 exec, %3\n\tglobal_load_lds_dword %0, %1\n\ts_mov_b64 exec, -1" ::"v"(off), "s"(base), "s"(lds), "s"(mask) : "memory");
}
#define SB __builtin_amdgcn_sched_barrier(0)
template <int N> DEV void wait_vm() { asm volatile("s_waitcnt vmcnt(%0)" ::"i"(N) : "memory"); }
DEV void bar() { asm volatile("s_waitcnt lgkmcnt(0)\n\ts_barrier" ::: "memory"); }
DEV bf8 lds128(unsigned a) { return *(LAS bf8*)(size_t)a; }
DEV u2 lds64(unsigned a) { return *(LAS u2*)(size_t)a; }
DEV void sts64(unsigned a, u2 v) { *(LAS u2*)(size_t)a = v; }
DEV void sts128(unsigned a, u4 v) { *(LAS u4*)(size_t)a = v; }
DEV bf4 ldtr(unsigned a) { return __builtin_amdgcn_ds_read_tr16_b64_v4bf16((LAS bf4*)(size_t)a); }
DEV bf8 cat(bf4 a, bf4 b) { return __builtin_shufflevector(a, b, 0, 1, 2, 3, 4, 5, 6, 7); }
DEV bf8 cat(u2 a, u2 b) { u4 r = {a.x, a.y, b.x, b.y}; return __builtin_bit_cast(bf8, r); }
DEV f4 mfma(bf8 a, bf8 b, f4 c) { return __builtin_amdgcn_mfma_f32_16x16x32_bf16(a, b, c, 0, 0, 0); }
DEV unsigned pk2(float a, float b) { f2 t = {a, b}; return __builtin_bit_cast(unsigned, __builtin_convertvector(t, bf2)); }
DEV u2 pk4(f4 v) { u2 r = {pk2(v[0], v[1]), pk2(v[2], v[3])}; return r; }
DEV float fmx(float a, float b) { return __builtin_elementwise_maximum(a, b); }
DEV float fmn(float a, float b) { return __builtin_elementwise_minimum(a, b); }
DEV float phi(float t) {
    float e = __builtin_amdgcn_exp2f(fmn(t * 1.4426950408889634f, 0.f));
    return (fmx(t, 0.f) + e) + 1e-6f;
}
DEV f4 phi4(f4 v) { f4 r = {phi(v[0]), phi(v[1]), phi(v[2]), phi(v[3])}; return r; }
DEV u64 mix64(u64 z) {
    z += 0x9E3779B97F4A7C15ull;
    z = (z ^ (z >> 30)) * 0xBF58476D1CE4E5B9ull;
    z = (z ^ (z >> 27)) * 0x94D049BB133111EBull;
    return z ^ (z >> 31);
}
DEV u64 ld_agent(const char* p) { return __hip_atomic_load((GAS u64*)p, __ATOMIC_RELAXED, __HIP_MEMORY_SCOPE_AGENT); }
DEV void st_agent(char* p, u64 v) { __hip_atomic_store((GAS u64*)p, v, __ATOMIC_RELAXED, __HIP_MEMORY_SCOPE_AGENT); }
DEV void poll_all(const char* p, u64 tag) {
#pragma unroll 1
    for (unsigned spins = 0; spins < (1u << 18); ++spins) {
        u64 v = ld_agent(p);
        if (__all(v == tag)) break;
        __builtin_amdgcn_s_sleep(1);
    }
    __builtin_amdgcn_fence(__ATOMIC_ACQUIRE, "wavefront");
}
DEV unsigned fb(float x) { return __builtin_bit_cast(unsigned, x); }
DEV float bf(unsigned x) { return __builtin_bit_cast(float, x); }
DEV unsigned opaque(unsigned x) { asm volatile("" : "+v"(x)); return x; }
DEV void rowmax4x4(float (&m)[4]) {
    auto a = __builtin_amdgcn_permlane16_swap(fb(m[0]), fb(m[1]), false, false);
    auto b = __builtin_amdgcn_permlane16_swap(fb(m[2]), fb(m[3]), false, false);
    float u = fmx(bf(a[0]), bf(a[1])), w = fmx(bf(b[0]), bf(b[1]));
    auto d = __builtin_amdgcn_permlane32_swap(fb(u), fb(w), false, false);
    unsigned t = fb(fmx(bf(d[0]), bf(d[1])));
    auto e = __builtin_amdgcn_permlane32_swap(t, opaque(t), false, false);
    auto f = __builtin_amdgcn_permlane16_swap(e[0], opaque(e[0]), false, false);
    auto h = __builtin_amdgcn_permlane16_swap(e[1], opaque(e[1]), false, false);
    m[0] = bf(f[0]); m[1] = bf(f[1]); m[2] = bf(h[0]); m[3] = bf(h[1]);
}

#define LEAVECLK "s_memrealtime"
#define LBAR(idx) bar()
#define LDUMP()
#define STAMP(i)
#define CSTAMP(i)
#define SDUMP()

DEV void store_o(__amdgpu_buffer_rsrc_t rs, unsigned obase, int par, int lane, const u2 (&t)[5]) {
    u4 a = {t[0].x, t[0].y, t[1].x, t[1].y}, b = {t[2].x, t[2].y, t[3].x, t[3].y};
    __builtin_amdgcn_raw_buffer_store_b128(a, rs, lane * 16, obase + par * 2048, 16);
    __builtin_amdgcn_raw_buffer_store_b128(b, rs, 1024 + lane * 16, obase + par * 2048, 16);
    __builtin_amdgcn_raw_buffer_store_b64(t[4], rs, lane * 8, obase + 4096 + par * 512, 16);
}

struct AttnMask { f4 co, cp; unsigned mk01, mk23; unsigned qlo[4], qhi[4]; };
DEV AttnMask attn_mask(bool prev_ok, int lane) {
    const int c = lane & 15, g = lane >> 4;
    AttnMask k;
    const float ninf = -__builtin_inff();
#pragma unroll
    for (int r = 0; r < 4; ++r) {
        const int d = c - (4 * g + r);
        k.co[r] = (unsigned)d <= 7u ? 0.f : ninf;
        k.cp[r] = (prev_ok & (d <= -9)) ? 0.f : ninf;
    }
    k.mk01 = (4 * g <= c ? 0xFFFFu : 0u) | (4 * g + 1 <= c ? 0xFFFF0000u : 0u);
    k.mk23 = (4 * g + 2 <= c ? 0xFFFFu : 0u) | (4 * g + 3 <= c ? 0xFFFF0000u : 0u);
#pragma unroll
    for (int h = 0; h < 4; ++h) { k.qlo[h] = g >= h ? ~0u : 0u; k.qhi[h] = g <= h ? ~0u : 0u; }
    return k;
}
DEV void attn_tile(const u2 (&q)[5], const u2 (&ko)[5], const u2 (&kp)[5], const bf8 (&vf)[5], const AttnMask& k, int lane, u2 (&out)[5]) {
    const int g = lane >> 4;
    const float sc = 0.22360679774997896f * 1.4426950408889634f;
    const u4 ones4 = {0x3F803F80u, 0x3F803F80u, 0x3F803F80u, 0x3F803F80u};
    const bf8 ones = __builtin_bit_cast(bf8, ones4);
    const f4 z4 = {0.f, 0.f, 0.f, 0.f};
    f4 so[4], sp[4], s[4];
    float m[4], rs[4];
    bf8 P[4];
#pragma unroll
    for (int h = 0; h < 4; ++h) {
        u2 a = q[h], b = q[h + 1];
        if (h > 0) { a.x &= k.qlo[h]; a.y &= k.qlo[h]; }
        if (h < 3) { b.x &= k.qhi[h]; b.y &= k.qhi[h]; }
        bf8 qf = cat(a, b);
        so[h] = mfma(cat(ko[h], ko[h + 1]), qf, k.co);
        sp[h] = mfma(cat(kp[h], kp[h + 1]), qf, k.cp);
    }
#pragma unroll
    for (int h = 0; h < 4; ++h) {
        s[h] = __builtin_elementwise_maximum(so[h], sp[h]);
        m[h] = fmx(fmx(s[h][0], s[h][1]), fmx(s[h][2], s[h][3]));
    }
    rowmax4x4(m);
#pragma unroll
    for (int h = 0; h < 4; ++h) {
        float p[4];
        const float ms = m[h] * sc;
#pragma unroll
        for (int r = 0; r < 4; ++r) p[r] = __builtin_amdgcn_exp2f(__builtin_fmaf(s[h][r], sc, -ms));
        const unsigned p01 = pk2(p[0], p[1]), p23 = pk2(p[2], p[3]);
        u2 pp = {p01 & ~k.mk01, p23 & ~k.mk23};
        u2 po = {p01 & k.mk01, p23 & k.mk23};
        P[h] = cat(pp, po);
    }
    f4 sm[4], oa[5], ob[3];
#pragma unroll
    for (int h = 0; h < 4; ++h) sm[h] = mfma(ones, P[h], z4);
    oa[0] = mfma(vf[0], P[0], z4);
#pragma unroll
    for (int i = 1; i < 4; ++i) { oa[i] = mfma(vf[i], P[i - 1], z4); ob[i - 1] = mfma(vf[i], P[i], z4); }
    oa[4] = mfma(vf[4], P[3], z4);
#pragma unroll
    for (int h = 0; h < 4; ++h) rs[h] = __builtin_amdgcn_rcpf(sm[h][0]);
    out[0] = pk4(oa[0] * rs[0]);
#pragma unroll
    for (int i = 1; i < 4; ++i) {
        const bool lo = g < i;
        const f4 o = lo ? oa[i] : ob[i - 1];
        out[i] = pk4(o * (lo ? rs[i - 1] : rs[i]));
    }
    out[4] = pk4(oa[4] * rs[3]);
}

#define FRONTV 1
constexpr int halo_owner(int n) { return n < 5 ? -1 : (n < 8 ? 0 : (n < 11 ? 1 : (n < 13 ? 2 : 3))); }
constexpr int halo_first(int cw) { return cw == 0 ? 5 : cw == 1 ? 8 : cw == 2 ? 11 : 13; }

__global__ __launch_bounds__(512, 2) void fused_kernel(const char* __restrict__ xA, const char* __restrict__ wA, const char* __restrict__ woA,
                                                       const char* __restrict__ xB, const char* __restrict__ wB, const char* __restrict__ woB,
                                                       char* __restrict__ out, char* __restrict__ ws) {
    extern __shared__ __attribute__((aligned(16))) char smem[];
    const int bid = blockIdx.x, xcd = bid & 7, j = bid >> 3;
    const int br = xcd >> 2, xq = xcd & 3;
    const int rt = (xq >> 1) * 8 + (j & 7), hg = (xq & 1) * 4 + (j >> 3);
    const int rt2 = xq * 4 + (j & 3), ct = j >> 2;
    const int tid = threadIdx.x, lane = tid & 63;
    const int wv = __builtin_amdgcn_readfirstlane(tid >> 6);
    const int cw = wv & 3, par = wv >> 2;
    const int c = lane & 15, g = lane >> 4;
    const f4 z4 = {0.f, 0.f, 0.f, 0.f};
    const u2 zero2 = {0u, 0u};
    unsigned lst = 0, sst = 0;
    STAMP(0);
    u64 tag;
    u2 q[5], ko[5], vo[5];
    const __amdgpu_buffer_rsrc_t rs = __builtin_amdgcn_make_buffer_rsrc(ws, 0, WS_TOTAL, 0x00020000);
    const unsigned ea = EX_OFF + (2 * cw + par) * 5120 + lane * 8;
    const unsigned qa = EX_OFF + (100 + cw * 5) * 512 + lane * 8 - (cw == 3 ? 35 * 512 : 0);

    if (__builtin_expect(wv >= 4, 1)) {
        const int lw = cw;
        __builtin_amdgcn_s_setprio(3);
        const char* x = br ? xB : xA;
        const char* w = br ? wB : wA;
#define KORD 2
        u64 seq;
        if (KORD == 0) seq = 0x9876543210ull;
        else if (KORD == 1) {
            int ph = ((5 * (j & 7)) >> 2) + ((5 * (j >> 3)) >> 1); if (ph >= 10) ph -= 10;
            seq = (0x9876543210ull >> (4 * ph)) | (0x9876543210ull << (4 * (10 - ph)));
        } else {
            int cl = ((j >> 3) - j) & 3;
            seq = cl == 0 ? 0x9765832140ull : cl == 1 ? 0x8769403521ull : cl == 2 ? 0x9875416032ull : 0x9865472103ull;
        }
        const unsigned voffx = (rt * 128 + (lw >> 1) + 16 * (lw & 1) + 2 * (lane >> 3)) * 1280 + (((lane & 7) ^ (4 * (lw & 1) + (lane >> 4))) & 7) * 16;
        const int r1 = lane >= 30, cc = lane - 30 * r1;
        const unsigned voffw = ((lw >> 1) * 8 + (lw & 1) * 2 + r1) * 3840 + ((cc * 26) >> 8) * 1120 + hg * 160 + cc * 16;
        const bool halo = (br == 0) & (lw < 2);
        const int hdelta = rt ? (-15 - 17 * lw) * 1280 : 0;
        auto stage = [&](int t, unsigned sl) {
            const int ks = (int)(seq >> (4 * t)) & 15;
            const char* xb = x + ks * 128;
            const char* wb = w + (size_t)ks * (64 * 3840);
#pragma unroll
            for (int i = 0; i < 4; ++i) glds16(xb + i * 40960, voffx, sl + 2048 + lw * 1024 + i * 4096);
            if (halo) glds16(xb + hdelta, voffx, sl + lw * 1024);
#pragma unroll
            for (int i = 0; i < 8; ++i) glds16m(wb + ((i >> 1) * 16 + (i & 1) * 4) * 3840, voffw, sl + XB + lw * 960 + i * 3840, (1ull << 60) - 1);
        };
        STAMP(9);
        stage(0, 2 * STB); stage(1, 0);
        STAMP(10);
        const char* wo = br ? woB : woA;
        const int wr = (lane * 26) >> 8;
        const unsigned voffo = wr * 1120 + lane * 16 + ct * 160 + lw * 6400;
        auto wo_load = [&](int b0, int n) {
#pragma unroll 1
            for (int i = b0; i < b0 + n; ++i) glds16m(wo + i * 25600, voffo, WO_OFF + lw * 800 + i * 3200, (1ull << 50) - 1);
        };
        unsigned nslot = STB;
#pragma unroll 1
        for (int t = 0; t < 8; ++t) {
            if (halo) wait_vm<13>(); else wait_vm<12>();
            LBAR(t);
            stage(t + 2, nslot);
            nslot += STB; if (nslot == 3 * STB) nslot = 0;
        }
        touch4m(wo + (j * 200 + lw * 50) * 128, lane * 128, DUMMY_OFF + lw * 256, (1ull << 50) - 1);
        if (halo) wait_vm<14>(); else wait_vm<13>();
        LBAR(8);
        wo_load(0, 11);
        wait_vm<12>();
        LBAR(9);
        wo_load(11, 10);
        LBAR(10);
        wo_load(21, 11);
        __builtin_amdgcn_s_setprio(0);
        tag = *(LAS u64*)(size_t)TAG_OFF;
        STAMP(1);
        bar();
        if (br == 0) {
#pragma unroll
            for (int i = 0; i < 5; ++i) {
                q[i] = lds64(qa + i * 512);
                ko[i] = lds64(ea + 10240 + i * 512);
                vo[i] = lds64(ea + 10240 + (5 + i) * 512);
            }
        }
    } else {
        tag = mix64(ld_agent(ws + WS_EPOCH));
        if (tid == 0) *(LAS u64*)(size_t)TAG_OFF = tag;
        f4 acc[2][15], ha[3];
#pragma unroll
        for (int m = 0; m < 2; ++m)
#pragma unroll
            for (int n = 0; n < 15; ++n) acc[m][n] = z4;
        ha[0] = ha[1] = ha[2] = z4;
        const unsigned wlane = XB + (16 * (g >> 1) + 4 * (g & 1) + ((lane >> 2) & 3)) * 480 + (lane & 3) * 8;
        const unsigned xh0 = c * 128 + ((g ^ (c >> 1)) & 7) * 16, xh1 = xh0 ^ 64;
        const unsigned xoff = (1 + 2 * cw) * 2048;
        auto mainloop = [&](auto MC) {
            constexpr int MODE = decltype(MC)::value;
            constexpr int BR = MODE == 4;
            bf8 wA_[5], wB_[5], xk0[3], xk1[3];
            auto chunk = [&](auto CHC, auto FRONT, auto RDX, const bf8 (&cur)[5], const bf8 (&xc)[3], bf8 (&nxt)[5], unsigned na, bf8 (&xn)[3], unsigned xa) {
                constexpr int CH = decltype(CHC)::value;
                constexpr bool Y = CH < (BR ? 1 : 2);
                bf4 lo[5], hi[5];
#pragma unroll
                for (int i = 0; i < 5; ++i) {
                    acc[0][CH * 5 + i] = Y ? mfma(cur[i], xc[0], acc[0][CH * 5 + i]) : mfma(xc[0], cur[i], acc[0][CH * 5 + i]);
                    if constexpr (decltype(FRONT)::value) { if (2 * i < 5) { lo[2 * i] = ldtr(na + 2 * i * 32); hi[2 * i] = ldtr(na + 2 * i * 32 + 3840); } }
                    else { if (decltype(RDX)::value && i == 0) xn[0] = lds128(xa + xoff); lo[i] = ldtr(na + i * 32); }
                    SB;
                    acc[1][CH * 5 + i] = Y ? mfma(cur[i], xc[1], acc[1][CH * 5 + i]) : mfma(xc[1], cur[i], acc[1][CH * 5 + i]);
                    if constexpr (decltype(FRONT)::value) { if (2 * i + 1 < 5) { lo[2 * i + 1] = ldtr(na + (2 * i + 1) * 32); hi[2 * i + 1] = ldtr(na + (2 * i + 1) * 32 + 3840); } }
                    else { hi[i] = ldtr(na + i * 32 + 3840); if (decltype(RDX)::value && i == 0) xn[1] = lds128(xa + xoff + 2048); if (decltype(RDX)::value && i == 4 && !BR) xn[2] = lds128(xa); }
                    SB;
                    if (!BR && halo_owner(CH * 5 + i) == MODE) {
                        const int hi_ = CH * 5 + i - halo_first(MODE & 3);
                        ha[hi_] = Y ? mfma(cur[i], xc[2], ha[hi_]) : mfma(xc[2], cur[i], ha[hi_]);
                        SB;
                    }
                }
#pragma unroll
                for (int i = 0; i < 5; ++i) nxt[i] = cat(lo[i], hi[i]);
            };
            unsigned slot = 2 * STB;
#pragma unroll
            for (int n = 0; n < 15; ++n) asm volatile("" : "+v"(acc[0][n]), "+v"(acc[1][n]));
            LBAR(0);
            STAMP(12); CSTAMP(13);
            xk0[0] = lds128(slot + xh0 + xoff); xk0[1] = lds128(slot + xh0 + xoff + 2048);
#pragma unroll
            for (int i = 0; i < 5; ++i) wA_[i] = cat(ldtr(slot + wlane + i * 32), ldtr(slot + wlane + i * 32 + 3840));
            if (!BR) xk0[2] = lds128(slot + xh0);
            SB;
#pragma unroll 1
            for (int t = 0; t < 10; ++t) {
                const unsigned wa = slot + wlane;
                chunk(IC<0>{}, IC<0>{}, IC<0>{}, wA_, xk0, wB_, wa + 160, xk1, 0u);
                chunk(IC<1>{}, IC<0>{}, IC<0>{}, wB_, xk0, wA_, wa + 320, xk1, 0u);
                chunk(IC<2>{}, IC<0>{}, IC<1>{}, wA_, xk0, wB_, wa + 15360, xk1, slot + xh1);
                chunk(IC<0>{}, IC<0>{}, IC<0>{}, wB_, xk1, wA_, wa + 15360 + 160, xk0, 0u);
                chunk(IC<1>{}, IC<FRONTV>{}, IC<0>{}, wA_, xk1, wB_, wa + 15360 + 320, xk0, 0u);
                slot += STB; if (slot == 3 * STB) slot = 0;
                LBAR(t + 1);
                SB;
                chunk(IC<2>{}, IC<0>{}, IC<1>{}, wB_, xk1, wA_, slot + wlane, xk0, slot + xh0);
            }
            if constexpr (!BR) {
#pragma unroll
                for (int n = 5; n < 15; ++n)
                    if (halo_owner(n) == MODE) {
                        const u2 v = pk4(ha[n - halo_first(MODE & 3)]);
                        sts64(EX_OFF + (n - 5) * 512 + lane * 8, v);
                        sts64(EX_OFF + (10 + n - 5) * 512 + (lane ^ (n < 10 ? 8 : 32)) * 8, v);
                    }
            }
        };
        if (br) mainloop(IC<4>{});
        else if (cw == 0) mainloop(IC<0>{});
        else if (cw == 1) mainloop(IC<1>{});
        else if (cw == 2) mainloop(IC<2>{});
        else mainloop(IC<3>{});
        STAMP(1); CSTAMP(14);
        if (br == 0) {
#pragma unroll
            for (int i = 0; i < 5; ++i) {
                sts64(qa + i * 512, pk4(acc[1][i]));
                sts64(ea + 15360 + i * 512, pk4(acc[1][5 + i]));
                sts64(ea + 15360 + (5 + i) * 512, pk4(acc[1][10 + i]));
            }
#pragma unroll
            for (int i = 0; i < 5; ++i) { ko[i] = pk4(acc[0][5 + i]); vo[i] = pk4(acc[0][10 + i]); }
            if (cw < 3) {
#pragma unroll
                for (int i = 0; i < 5; ++i) { sts64(ea + 10240 + i * 512, ko[i]); sts64(ea + 10240 + (5 + i) * 512, vo[i]); }
            }
            bar();
#pragma unroll
            for (int i = 0; i < 5; ++i) q[i] = pk4(acc[0][i]);
        } else {
#pragma unroll
            for (int i = 0; i < 5; ++i) {
                u2 a2 = pk4(acc[0][10 + i]), b2 = pk4(acc[1][10 + i]);
                u4 t2 = {a2.x, a2.y, b2.x, b2.y};
                sts128(EX_OFF + (cw * 10 + 5 + i) * 1024 + lane * 16, t2);
            }
#pragma unroll
            for (int i = 0; i < 5; ++i) {
                u2 a = pk4(phi4(acc[0][5 + i])), b = pk4(phi4(acc[1][5 + i]));
                u4 t = {a.x, a.y, b.x, b.y};
                sts128(EX_OFF + (cw * 10 + i) * 1024 + lane * 16, t);
            }
            bar();
#pragma unroll
            for (int i = 0; i < 5; ++i) { q[i] = pk4(phi4(acc[0][i])); ko[i] = pk4(phi4(acc[1][i])); }
        }
    }
    STAMP(2);
    const unsigned obase = WS_O + (((br * 16 + rt) * 4 + cw) * 8 + hg) * 5120;
    u2 o[5];
    if (br == 0) {
        const AttnMask mk = attn_mask((rt | cw) != 0, lane);
        u2 kp[5];
        bf8 vf[5];
#pragma unroll
        for (int i = 0; i < 5; ++i) kp[i] = lds64(ea + i * 512);
#pragma unroll
        for (int i = 0; i < 5; ++i) vf[i] = cat(lds64(ea + (5 + i) * 512), vo[i]);
        attn_tile(q, ko, kp, vf, mk, lane, o);
        STAMP(3); STAMP(4);
    } else {
        if (wv >= 3) {
            const u4 ones4 = {0x3F803F80u, 0x3F803F80u, 0x3F803F80u, 0x3F803F80u};
            const bf8 ones = __builtin_bit_cast(bf8, ones4);
            const unsigned pbase = WS_KVP + (hg * 16 + rt) * KVP_STRIDE;
            const int dt = wv == 3 ? 4 : wv - 4;
            f4 t[3] = {z4, z4, z4}, tz = z4;
            const int e0 = max(dt - 1, 0), e2 = min(dt + 1, 4);
#pragma unroll
            for (int s = 0; s < 4; ++s) {
                bf8 kf = lds128(EX_OFF + (s * 10 + dt) * 1024 + lane * 16);
                t[0] = mfma(kf, lds128(EX_OFF + (s * 10 + 5 + e0) * 1024 + lane * 16), t[0]);
                t[1] = mfma(kf, lds128(EX_OFF + (s * 10 + 5 + dt) * 1024 + lane * 16), t[1]);
                t[2] = mfma(kf, lds128(EX_OFF + (s * 10 + 5 + e2) * 1024 + lane * 16), t[2]);
                tz = mfma(kf, ones, tz);
            }
            const int Q = 4 * dt + g;
#pragma unroll
            for (int a = 0; a < 3; ++a) {
                int et = dt - 1 + a;
                int e = 16 * et + c, h = (e * 205) >> 12, u = Q - 5 * h;
                unsigned off = ((unsigned)et <= 4u && (unsigned)u <= 4u) ? pbase + (u * 80 + e) * 16 : 0xF0000000u;
                __builtin_amdgcn_raw_buffer_store_b128(__builtin_bit_cast(u4, t[a]), rs, off, 0, 16);
            }
            __builtin_amdgcn_raw_buffer_store_b128(__builtin_bit_cast(u4, tz), rs, c == 0 ? pbase + (400 + Q) * 16 : 0xF0000000u, 0, 16);
        }
        {
            const u4 zz = {0u, 0u, 0u, 0u};
            sts128(IMG_OFF + tid * 16, zz); sts128(IMG_OFF + 8192 + tid * 16, zz);
        }
        wait_vm<0>();
        bar();
        if (wv == 4) {
            if (lane == 0) st_agent(ws + WS_FKV + (hg * 16 + rt) * 128, tag);
            poll_all(ws + WS_FKV + (hg * 16 + c) * 128, tag);
        }
        if (wv < 4) {
#pragma unroll
            for (int i = 0; i < 5; ++i) sts64(PQ_OFF + (cw * 5 + i) * 512 + lane * 8, ko[i]);
        }
        bar();
        STAMP(3);
        if (tid < 420) {
            f4 s = z4;
            u4 v[16];
#pragma unroll
            for (int r = 0; r < 16; ++r) v[r] = __builtin_amdgcn_raw_buffer_load_b128(rs, tid * 16, WS_KVP + (hg * 16 + ((r + 2 * rt) & 15)) * KVP_STRIDE, 16);
#pragma unroll
            for (int r = 0; r < 16; ++r) s += __builtin_bit_cast(f4, v[r]);
            int u = tid / 80, e = tid - u * 80, h = e / 20;
            unsigned a = tid < 400 ? e * 176 + (5 * h + u) * 8 : (80 + (tid - 400) / 5) * 176 + (tid - 400) * 8;
            sts64(IMG_OFF + a, pk4(s));
        }
        if (wv >= 4) {
#pragma unroll
            for (int i = 0; i < 5; ++i) q[i] = lds64(PQ_OFF + (cw * 5 + i) * 512 + lane * 8);
        }
        bar();
        STAMP(4);
        const unsigned ia = IMG_OFF + c * 176 + g * 8;
        const unsigned za = IMG_OFF + (80 + (c & 3)) * 176 + g * 8;
        bf8 zf0 = cat(lds64(za), lds64(za + 32)), zf1 = cat(lds64(za + 64), lds64(za + 96)), zf2 = cat(lds64(za + 128), zero2);
        bf8 kf[9];
        kf[0] = cat(lds64(ia), lds64(ia + 32));
        kf[1] = cat(lds64(ia + 2816), lds64(ia + 2816 + 32));
        kf[2] = cat(lds64(ia + 2816 + 64), lds64(ia + 2816 + 96));
        kf[3] = cat(lds64(ia + 2 * 2816), lds64(ia + 2 * 2816 + 32));
        kf[4] = cat(lds64(ia + 2 * 2816 + 64), lds64(ia + 2 * 2816 + 96));
        kf[5] = cat(lds64(ia + 3 * 2816 + 64), lds64(ia + 3 * 2816 + 96));
        kf[6] = cat(lds64(ia + 3 * 2816 + 128), zero2);
        kf[7] = cat(lds64(ia + 4 * 2816 + 64), lds64(ia + 4 * 2816 + 96));
        kf[8] = cat(lds64(ia + 4 * 2816 + 128), zero2);
        bf8 p01 = cat(q[0], q[1]), p23 = cat(q[2], q[3]), p4 = cat(q[4], zero2);
        f4 den = mfma(zf2, p4, mfma(zf1, p23, mfma(zf0, p01, z4)));
        f4 n0 = mfma(kf[0], p01, z4);
        f4 n1 = mfma(kf[2], p23, mfma(kf[1], p01, z4));
        f4 n2 = mfma(kf[4], p23, mfma(kf[3], p01, z4));
        f4 n3 = mfma(kf[6], p4, mfma(kf[5], p23, z4));
        f4 n4 = mfma(kf[8], p4, mfma(kf[7], p23, z4));
        float r0 = __builtin_amdgcn_rcpf(den[0] + 1e-6f), r1 = __builtin_amdgcn_rcpf(den[1] + 1e-6f);
        float r2 = __builtin_amdgcn_rcpf(den[2] + 1e-6f), r3 = __builtin_amdgcn_rcpf(den[3] + 1e-6f);
        o[0] = pk4(n0 * r0);
        o[1] = pk4(n1 * (g < 1 ? r0 : r1));
        o[2] = pk4(n2 * (g < 2 ? r1 : r2));
        o[3] = pk4(n3 * (g < 3 ? r2 : r3));
        o[4] = pk4(n4 * r3);
    }
    store_o(rs, obase, par, lane, o);
    wait_vm<0>();
    STAMP(11);
    bar();
    if (tid == 0) st_agent(ws + WS_FO + ((br * 16 + rt) * 8 + hg) * 128, tag);
    STAMP(5);

    {
        const unsigned wl = WO_OFF + (lane >> 2) * 160 + (lane & 3) * 8;
        unsigned gb[4], so[4];
        const unsigned ob = WS_O + ((br * 16 + rt2) * 4 + cw) * 8 * 5120;
#pragma unroll
        for (int gg = 0; gg < 4; ++gg) { const int gr = (gg + ct + 4 * par) & 7; gb[gg] = wl + gr * 12800; so[gg] = ob + gr * 5120; }
        bf8 wA_[5], wB_[5];
#pragma unroll
        for (int i = 0; i < 5; ++i) wA_[i] = cat(ldtr(gb[0] + i * 32), ldtr(gb[0] + i * 32 + 16 * 160));
        f4 a2[2][5];
#pragma unroll
        for (int n = 0; n < 5; ++n) { a2[0][n] = z4; a2[1][n] = z4; asm volatile("" : "+v"(a2[0][n]), "+v"(a2[1][n])); }
        poll_all(ws + WS_FO + ((br * 16 + rt2) * 8 + ((ct + 4 * par + (lane & 3)) & 7)) * 128, tag);
        STAMP(6);
        u4 of[4][4];
        u2 ot[4][2];
        auto ld = [&](auto SC, int half) {
            constexpr int s = decltype(SC)::value;
            if constexpr (s < 10) {
                constexpr int u = s / 5, r = s % 5;
                if constexpr (r < 4) {
                    constexpr int gg = 2 * u + (r >> 1), pr = r & 1;
                    of[gg][2 * half + pr] = __builtin_amdgcn_raw_buffer_load_b128(rs, half * 2048 + pr * 1024 + lane * 16, so[gg], 16);
                } else {
                    ot[2 * u][half] = __builtin_amdgcn_raw_buffer_load_b64(rs, half * 512 + lane * 8, so[2 * u] + 4096, 16);
                    ot[2 * u + 1][half] = __builtin_amdgcn_raw_buffer_load_b64(rs, half * 512 + lane * 8, so[2 * u + 1] + 4096, 16);
                }
            }
        };
#define P2AHEAD 3
        ld(IC<0>{}, 0); ld(IC<0>{}, 1); ld(IC<1>{}, 0); ld(IC<1>{}, 1);
        if (P2AHEAD > 2) { ld(IC<2>{}, 0); ld(IC<2>{}, 1); }
        if (P2AHEAD > 3) { ld(IC<3>{}, 0); ld(IC<3>{}, 1); }
        SB;
#define B8(x) __builtin_bit_cast(bf8, x)
        auto step = [&](auto SC, const bf8 (&cur)[5], bf8 (&nxt)[5]) {
            constexpr int s = decltype(SC)::value;
            constexpr int u = s / 5, r = s % 5;
            bf8 o0, o1;
            if constexpr (r < 4) { o0 = B8(of[2 * u + (r >> 1)][r & 1]); o1 = B8(of[2 * u + (r >> 1)][2 + (r & 1)]); }
            else { o0 = cat(ot[2 * u][0], ot[2 * u + 1][0]); o1 = cat(ot[2 * u][1], ot[2 * u + 1][1]); }
            constexpr int s1 = s + 1, u1 = (s1 / 5) & 1, r1 = s1 % 5;
            const unsigned na = r1 < 4 ? gb[2 * u1 + ((r1 >> 1) & 1)] + (r1 & 1) * 32 * 160 : gb[2 * u1] + 64 * 160;
            const unsigned nb = r1 < 4 ? na + 16 * 160 : gb[2 * u1 + 1] + 64 * 160;
            bf4 lo[5], hi[5];
#pragma unroll
            for (int i = 0; i < 5; ++i) {
                a2[0][i] = mfma(cur[i], o0, a2[0][i]); if (s < 9) lo[i] = ldtr(na + i * 32);
                if (i == 1) ld(IC<s + P2AHEAD>{}, 0);
                if (i == 3) ld(IC<s + P2AHEAD>{}, 1);
                SB;
                a2[1][i] = mfma(cur[i], o1, a2[1][i]); if (s < 9) hi[i] = ldtr(nb + i * 32); SB;
            }
            if (s < 9) {
#pragma unroll
                for (int i = 0; i < 5; ++i) nxt[i] = cat(lo[i], hi[i]);
            }
        };
        step(IC<0>{}, wA_, wB_); step(IC<1>{}, wB_, wA_); step(IC<2>{}, wA_, wB_); step(IC<3>{}, wB_, wA_); step(IC<4>{}, wA_, wB_);
        step(IC<5>{}, wB_, wA_); step(IC<6>{}, wA_, wB_); step(IC<7>{}, wB_, wA_); step(IC<8>{}, wA_, wB_); step(IC<9>{}, wB_, wA_);
        STAMP(7);
#pragma unroll
        for (int n = 0; n < 5; ++n) sts128(EX_OFF + (wv * 5 + n) * 1024 + lane * 16, __builtin_bit_cast(u4, par ? a2[0][n] : a2[1][n]));
        bar();
        char* op = out + (size_t)(rt2 * 128 + 32 * cw + 2 * c + par) * 2560 + br * 1280 + ct * 160 + g * 8;
#pragma unroll
        for (int n = 0; n < 5; ++n) {
            const f4 other = *(LAS f4*)(size_t)(EX_OFF + ((wv ^ 4) * 5 + n) * 1024 + lane * 16);
            const f4 mine = par ? a2[1][n] : a2[0][n];
            *(GAS u2*)(op + n * 32) = pk4(par ? other + mine : mine + other);
        }
        STAMP(8);
        LDUMP(); SDUMP();
    }
    if (bid == 0 && wv == 7) {
        const char* p = ws + WS_FO + lane * 128;
#pragma unroll 1
        for (unsigned spins = 0; spins < (1u << 18); ++spins) {
            const u64 v0 = ld_agent(p), v1 = ld_agent(p + 8192), v2 = ld_agent(p + 16384), v3 = ld_agent(p + 24576);
            if (__all((v0 == tag) & (v1 == tag) & (v2 == tag) & (v3 == tag))) break;
            __builtin_amdgcn_s_sleep(2);
        }
        if (lane == 0) st_agent(ws + WS_EPOCH, tag);
    }
}

extern "C" void kernel_launch(void* const* d_in, const int* in_sizes, int n_in, void* d_out, int out_size, void* d_ws, size_t ws_size,
                              hipStream_t stream) {
    const char *xA = (const char*)d_in[0], *wA = (const char*)d_in[1], *woA = (const char*)d_in[2];
    const char *xB = (const char*)d_in[3], *wB = (const char*)d_in[4], *woB = (const char*)d_in[5];
    (void)hipFuncSetAttribute((const void*)fused_kernel, hipFuncAttributeMaxDynamicSharedMemorySize, LDS_TOTAL);
    fused_kernel<<<256, 512, LDS_TOTAL, stream>>>(xA, wA, woA, xB, wB, woB, (char*)d_out, (char*)d_ws);
}
```

```cpp
#include <hip/hip_runtime.h>
#include <hip/hip_bf16.h>

typedef __bf16 bf8 __attribute__((ext_vector_type(8)));
typedef __bf16 bf4 __attribute__((ext_vector_type(4)));
typedef __bf16 bf2 __attribute__((ext_vector_type(2)));
typedef float f4 __attribute__((ext_vector_type(4)));
typedef float f2 __attribute__((ext_vector_type(2)));
typedef unsigned u4 __attribute__((ext_vector_type(4)));
typedef unsigned u2 __attribute__((ext_vector_type(2)));
typedef unsigned long long u64;
#define GAS __attribute__((address_space(1)))
#define LAS __attribute__((address_space(3)))
#define DEV __device__ __forceinline__
template <int V> struct IC { static constexpr int value = V; };

constexpr unsigned WS_EPOCH = 0;
constexpr unsigned WS_FKV = 4096;
constexpr unsigned WS_FO = WS_FKV + 128 * 128;
constexpr unsigned WS_KVP = 65536;
constexpr unsigned KVP_STRIDE = 7168;
constexpr unsigned WS_O = 1u << 20;
constexpr unsigned WS_STAMP = 8u << 20;
constexpr unsigned WS_TOTAL = 9u << 20;

constexpr unsigned XB = 144 * 128, WB = 64 * 480, STB = XB + WB;
constexpr unsigned WO_OFF = 0;
constexpr unsigned EX_OFF = 102400;
constexpr unsigned LDS_TOTAL = 163840;
constexpr unsigned TAG_OFF = LDS_TOTAL - 16;
constexpr unsigned IMG_OFF = EX_OFF + 40960;
constexpr unsigned DUMMY_OFF = 161792;
constexpr unsigned PQ_OFF = EX_OFF;

DEV void glds16(const char* base, unsigned off, unsigned lds) {
    asm volatile("s_mov_b32 m0, %2\n\ts_nop 0\n\tglobal_load_lds_dwordx4 %0, %1" ::"v"(off), "s"(base), "s"(lds) : "memory");
}
DEV void glds16m(const char* base, unsigned off, unsigned lds, u64 mask) {
    asm volatile("s_mov_b32 m0, %2\n\ts_mov_b64 exec, %3\n\tglobal_load_lds_dwordx4 %0, %1\n\ts_mov_b64 exec, -1" ::"v"(off), "s"(base), "s"(lds), "s"(mask) : "memory");
}
DEV void touch4m(const char* base, unsigned off, unsigned lds, u64 mask) {
    asm volatile("s_mov_b32 m0, %2\n\ts_mov_b64 exec, %3\n\tglobal_load_lds_dword %0, %1\n\ts_mov_b64 exec, -1" ::"v"(off), "s"(base), "s"(lds), "s"(mask) : "memory");
}
#define SB __builtin_amdgcn_sched_barrier(0)
template <int N> DEV void wait_vm() { asm volatile("s_waitcnt vmcnt(%0)" ::"i"(N) : "memory"); }
DEV void bar() { asm volatile("s_waitcnt lgkmcnt(0)\n\ts_barrier" ::: "memory"); }
DEV bf8 lds128(unsigned a) { return *(LAS bf8*)(size_t)a; }
DEV u2 lds64(unsigned a) { return *(LAS u2*)(size_t)a; }
DEV void sts64(unsigned a, u2 v) { *(LAS u2*)(size_t)a = v; }
DEV void sts128(unsigned a, u4 v) { *(LAS u4*)(size_t)a = v; }
DEV bf4 ldtr(unsigned a) { return __builtin_amdgcn_ds_read_tr16_b64_v4bf16((LAS bf4*)(size_t)a); }
DEV bf8 cat(bf4 a, bf4 b) { return __builtin_shufflevector(a, b, 0, 1, 2, 3, 4, 5, 6, 7); }
DEV bf8 cat(u2 a, u2 b) { u4 r = {a.x, a.y, b.x, b.y}; return __builtin_bit_cast(bf8, r); }
DEV f4 mfma(bf8 a, bf8 b, f4 c) { return __builtin_amdgcn_mfma_f32_16x16x32_bf16(a, b, c, 0, 0, 0); }
DEV unsigned pk2(float a, float b) { f2 t = {a, b}; return __builtin_bit_cast(unsigned, __builtin_convertvector(t, bf2)); }
DEV u2 pk4(f4 v) { u2 r = {pk2(v[0], v[1]), pk2(v[2], v[3])}; return r; }
DEV float fmx(float a, float b) { return __builtin_elementwise_maximum(a, b); }
DEV float fmn(float a, float b) { return __builtin_elementwise_minimum(a, b); }
DEV float phi(float t) {
    float e = __builtin_amdgcn_exp2f(fmn(t * 1.4426950408889634f, 0.f));
    return (fmx(t, 0.f) + e) + 1e-6f;
}
DEV f4 phi4(f4 v) { f4 r = {phi(v[0]), phi(v[1]), phi(v[2]), phi(v[3])}; return r; }
DEV u64 mix64(u64 z) {
    z += 0x9E3779B97F4A7C15ull;
    z = (z ^ (z >> 30)) * 0xBF58476D1CE4E5B9ull;
    z = (z ^ (z >> 27)) * 0x94D049BB133111EBull;
    return z ^ (z >> 31);
}
DEV u64 ld_agent(const char* p) { return __hip_atomic_load((GAS u64*)p, __ATOMIC_RELAXED, __HIP_MEMORY_SCOPE_AGENT); }
DEV void st_agent(char* p, u64 v) { __hip_atomic_store((GAS u64*)p, v, __ATOMIC_RELAXED, __HIP_MEMORY_SCOPE_AGENT); }
DEV void poll_all(const char* p, u64 tag) {
#pragma unroll 1
    for (unsigned spins = 0; spins < (1u << 18); ++spins) {
        u64 v = ld_agent(p);
        if (__all(v == tag)) break;
        __builtin_amdgcn_s_sleep(1);
    }
    __builtin_amdgcn_fence(__ATOMIC_ACQUIRE, "wavefront");
}
DEV unsigned fb(float x) { return __builtin_bit_cast(unsigned, x); }
DEV float bf(unsigned x) { return __builtin_bit_cast(float, x); }
DEV unsigned opaque(unsigned x) { asm volatile("" : "+v"(x)); return x; }
DEV void rowmax4x4(float (&m)[4]) {
    auto a = __builtin_amdgcn_permlane16_swap(fb(m[0]), fb(m[1]), false, false);
    auto b = __builtin_amdgcn_permlane16_swap(fb(m[2]), fb(m[3]), false, false);
    float u = fmx(bf(a[0]), bf(a[1])), w = fmx(bf(b[0]), bf(b[1]));
    auto d = __builtin_amdgcn_permlane32_swap(fb(u), fb(w), false, false);
    unsigned t = fb(fmx(bf(d[0]), bf(d[1])));
    auto e = __builtin_amdgcn_permlane32_swap(t, opaque(t), false, false);
    auto f = __builtin_amdgcn_permlane16_swap(e[0], opaque(e[0]), false, false);
    auto h = __builtin_amdgcn_permlane16_swap(e[1], opaque(e[1]), false, false);
    m[0] = bf(f[0]); m[1] = bf(f[1]); m[2] = bf(h[0]); m[3] = bf(h[1]);
}

#define LEAVECLK "s_memrealtime"
#define LBAR(idx) bar()
#define LDUMP()
#define STAMP(i)
#define CSTAMP(i)
#define SDUMP()

#define PAD(p)
DEV void store_o(__amdgpu_buffer_rsrc_t rs, unsigned obase, int par, int lane, const u2 (&t)[5]) {
    u4 a = {t[0].x, t[0].y, t[1].x, t[1].y}, b = {t[2].x, t[2].y, t[3].x, t[3].y};
    __builtin_amdgcn_raw_buffer_store_b128(a, rs, lane * 16, obase + par * 2048, 16);
    __builtin_amdgcn_raw_buffer_store_b128(b, rs, 1024 + lane * 16, obase + par * 2048, 16);
    __builtin_amdgcn_raw_buffer_store_b64(t[4], rs, lane * 8, obase + 4096 + par * 512, 16);
}

struct AttnMask { f4 co, cp; unsigned mk01, mk23; unsigned qlo[4], qhi[4]; };
DEV AttnMask attn_mask(bool prev_ok, int lane) {
    const int c = lane & 15, g = lane >> 4;
    AttnMask k;
    const float ninf = -__builtin_inff();
#pragma unroll
    for (int r = 0; r < 4; ++r) {
        const int d = c - (4 * g + r);
        k.co[r] = (unsigned)d <= 7u ? 0.f : ninf;
        k.cp[r] = (prev_ok & (d <= -9)) ? 0.f : ninf;
    }
    k.mk01 = (4 * g <= c ? 0xFFFFu : 0u) | (4 * g + 1 <= c ? 0xFFFF0000u : 0u);
    k.mk23 = (4 * g + 2 <= c ? 0xFFFFu : 0u) | (4 * g + 3 <= c ? 0xFFFF0000u : 0u);
#pragma unroll
    for (int h = 0; h < 4; ++h) { k.qlo[h] = g >= h ? ~0u : 0u; k.qhi[h] = g <= h ? ~0u : 0u; }
    return k;
}
DEV void attn_tile(const u2 (&q)[5], const u2 (&ko)[5], const u2 (&kp)[5], const bf8 (&vf)[5], const AttnMask& k, int lane, u2 (&out)[5]) {
    const int g = lane >> 4;
    const float sc = 0.22360679774997896f * 1.4426950408889634f;
    const u4 ones4 = {0x3F803F80u, 0x3F803F80u, 0x3F803F80u, 0x3F803F80u};
    const bf8 ones = __builtin_bit_cast(bf8, ones4);
    const f4 z4 = {0.f, 0.f, 0.f, 0.f};
    f4 so[4], sp[4], s[4];
    float m[4], rs[4];
    bf8 P[4];
#pragma unroll
    for (int h = 0; h < 4; ++h) {
        u2 a = q[h], b = q[h + 1];
        if (h > 0) { a.x &= k.qlo[h]; a.y &= k.qlo[h]; }
        if (h < 3) { b.x &= k.qhi[h]; b.y &= k.qhi[h]; }
        bf8 qf = cat(a, b);
        so[h] = mfma(cat(ko[h], ko[h + 1]), qf, k.co);
        sp[h] = mfma(cat(kp[h], kp[h + 1]), qf, k.cp);
    }
#pragma unroll
    for (int h = 0; h < 4; ++h) {
        s[h] = __builtin_elementwise_maximum(so[h], sp[h]);
        m[h] = fmx(fmx(s[h][0], s[h][1]), fmx(s[h][2], s[h][3]));
    }
    rowmax4x4(m);
#pragma unroll
    for (int h = 0; h < 4; ++h) {
        float p[4];
        const float ms = m[h] * sc;
#pragma unroll
        for (int r = 0; r < 4; ++r) p[r] = __builtin_amdgcn_exp2f(__builtin_fmaf(s[h][r], sc, -ms));
        const unsigned p01 = pk2(p[0], p[1]), p23 = pk2(p[2], p[3]);
        u2 pp = {p01 & ~k.mk01, p23 & ~k.mk23};
        u2 po = {p01 & k.mk01, p23 & k.mk23};
        P[h] = cat(pp, po);
    }
    f4 sm[4], oa[5], ob[3];
#pragma unroll
    for (int h = 0; h < 4; ++h) sm[h] = mfma(ones, P[h], z4);
    oa[0] = mfma(vf[0], P[0], z4);
#pragma unroll
    for (int i = 1; i < 4; ++i) { oa[i] = mfma(vf[i], P[i - 1], z4); ob[i - 1] = mfma(vf[i], P[i], z4); }
    oa[4] = mfma(vf[4], P[3], z4);
#pragma unroll
    for (int h = 0; h < 4; ++h) rs[h] = __builtin_amdgcn_rcpf(sm[h][0]);
    out[0] = pk4(oa[0] * rs[0]);
#pragma unroll
    for (int i = 1; i < 4; ++i) {
        const bool lo = g < i;
        const f4 o = lo ? oa[i] : ob[i - 1];
        out[i] = pk4(o * (lo ? rs[i - 1] : rs[i]));
    }
    out[4] = pk4(oa[4] * rs[3]);
}

#define FRONTV 1
constexpr int halo_owner(int n) { return n < 5 ? -1 : (n < 8 ? 0 : (n < 11 ? 1 : (n < 13 ? 2 : 3))); }
constexpr int halo_first(int cw) { return cw == 0 ? 5 : cw == 1 ? 8 : cw == 2 ? 11 : 13; }

__global__ __launch_bounds__(512, 2) void fused_kernel(const char* __restrict__ xA, const char* __restrict__ wA, const char* __restrict__ woA,
                                                       const char* __restrict__ xB, const char* __restrict__ wB, const char* __restrict__ woB,
                                                       char* __restrict__ out, char* __restrict__ ws) {
    extern __shared__ __attribute__((aligned(16))) char smem[];
    const int bid = blockIdx.x, xcd = bid & 7, j = bid >> 3;
    const int br = xcd >> 2, xq = xcd & 3;
    const int rt = (xq >> 1) * 8 + (j & 7), hg = (xq & 1) * 4 + (j >> 3);
    const int rt2 = xq * 4 + (j & 3), ct = j >> 2;
    const int tid = threadIdx.x, lane = tid & 63;
    const int wv = __builtin_amdgcn_readfirstlane(tid >> 6);
    const int cw = wv & 3, par = wv >> 2;
    const int c = lane & 15, g = lane >> 4;
    const f4 z4 = {0.f, 0.f, 0.f, 0.f};
    const u2 zero2 = {0u, 0u};
    unsigned lst = 0, sst = 0;
    STAMP(0);
    u64 tag;
    u2 q[5], ko[5], vo[5];
    const __amdgpu_buffer_rsrc_t rs = __builtin_amdgcn_make_buffer_rsrc(ws, 0, WS_TOTAL, 0x00020000);
    const unsigned ea = EX_OFF + (2 * cw + par) * 5120 + lane * 8;
    const unsigned qa = EX_OFF + (100 + cw * 5) * 512 + lane * 8 - (cw == 3 ? 35 * 512 : 0);

    if (__builtin_expect(wv >= 4, 1)) {
        const int lw = cw;
        __builtin_amdgcn_s_setprio(3);
        const char* x = br ? xB : xA;
        const char* w = br ? wB : wA;
#define KORD 1
        u64 seq;
        if (KORD == 0) seq = 0x9876543210ull;
        else if (KORD == 1) {
            int ph = ((5 * (j & 7)) >> 2) + ((5 * (j >> 3)) >> 1); if (ph >= 10) ph -= 10;
            seq = (0x9876543210ull >> (4 * ph)) | (0x9876543210ull << (4 * (10 - ph)));
        } else {
            int cl = ((j >> 3) - j) & 3;
            seq = cl == 0 ? 0x9765832140ull : cl == 1 ? 0x8769403521ull : cl == 2 ? 0x9875416032ull : 0x9865472103ull;
        }
        const unsigned voffx = (rt * 128 + (lw >> 1) + 16 * (lw & 1) + 2 * (lane >> 3)) * 1280 + (((lane & 7) ^ (4 * (lw & 1) + (lane >> 4))) & 7) * 16;
        const int r1 = lane >= 30, cc = lane - 30 * r1;
        const unsigned voffw = ((lw >> 1) * 8 + (lw & 1) * 2 + r1) * 3840 + ((cc * 26) >> 8) * 1120 + hg * 160 + cc * 16;
        const bool halo = (br == 0) & (lw < 2);
        const int hdelta = rt ? (-15 - 17 * lw) * 1280 : 0;
        auto stage = [&](int t, unsigned sl) {
            const int ks = (int)(seq >> (4 * t)) & 15;
            const char* xb = x + ks * 128;
            const char* wb = w + (size_t)ks * (64 * 3840);
#pragma unroll
            for (int i = 0; i < 4; ++i) glds16(xb + i * 40960, voffx, sl + 2048 + lw * 1024 + i * 4096);
            if (halo) glds16(xb + hdelta, voffx, sl + lw * 1024);
#pragma unroll
            for (int i = 0; i < 8; ++i) glds16m(wb + ((i >> 1) * 16 + (i & 1) * 4) * 3840, voffw, sl + XB + lw * 960 + i * 3840, (1ull << 60) - 1);
        };
        STAMP(9);
        stage(0, 2 * STB); stage(1, 0);
        STAMP(10);
        const char* wo = br ? woB : woA;
        const int wr = (lane * 26) >> 8;
        const unsigned voffo = wr * 1120 + lane * 16 + ct * 160 + lw * 6400;
        auto wo_load = [&](int b0, int n) {
#pragma unroll 1
            for (int i = b0; i < b0 + n; ++i) glds16m(wo + i * 25600, voffo, WO_OFF + lw * 800 + i * 3200, (1ull << 50) - 1);
        };
        unsigned nslot = STB;
#pragma unroll 1
        for (int t = 0; t < 8; ++t) {
            if (halo) wait_vm<13>(); else wait_vm<12>();
            LBAR(t);
            stage(t + 2, nslot);
            nslot += STB; if (nslot == 3 * STB) nslot = 0;
        }
        touch4m(wo + (j * 200 + lw * 50) * 128, lane * 128, DUMMY_OFF + lw * 256, (1ull << 50) - 1);
        if (halo) wait_vm<14>(); else wait_vm<13>();
        LBAR(8);
        wo_load(0, 11);
        wait_vm<12>();
        LBAR(9);
        wo_load(11, 10);
        LBAR(10);
        wo_load(21, 11);
        __builtin_amdgcn_s_setprio(0);
        tag = *(LAS u64*)(size_t)TAG_OFF;
        STAMP(1);
        bar();
        if (br == 0) {
#pragma unroll
            for (int i = 0; i < 5; ++i) {
                q[i] = lds64(qa + i * 512);
                ko[i] = lds64(ea + 10240 + i * 512);
                vo[i] = lds64(ea + 10240 + (5 + i) * 512);
            }
        }
    } else {
        tag = mix64(ld_agent(ws + WS_EPOCH));
        if (tid == 0) *(LAS u64*)(size_t)TAG_OFF = tag;
        f4 acc[2][15], ha[3];
#pragma unroll
        for (int m = 0; m < 2; ++m)
#pragma unroll
            for (int n = 0; n < 15; ++n) acc[m][n] = z4;
        ha[0] = ha[1] = ha[2] = z4;
        const unsigned wlane = XB + (16 * (g >> 1) + 4 * (g & 1) + ((lane >> 2) & 3)) * 480 + (lane & 3) * 8;
        const unsigned xh0 = c * 128 + ((g ^ (c >> 1)) & 7) * 16, xh1 = xh0 ^ 64;
        const unsigned xoff = (1 + 2 * cw) * 2048;
        auto mainloop = [&](auto MC) {
            constexpr int MODE = decltype(MC)::value;
            constexpr int BR = MODE == 4;
            bf8 wA_[5], wB_[5], xk0[3], xk1[3];
            auto chunk = [&](auto CHC, auto FRONT, auto RDX, const bf8 (&cur)[5], const bf8 (&xc)[3], bf8 (&nxt)[5], unsigned na, bf8 (&xn)[3], unsigned xa) {
                constexpr int CH = decltype(CHC)::value;
                constexpr bool Y = CH < (BR ? 1 : 2);
                bf4 lo[5], hi[5];
#pragma unroll
                for (int i = 0; i < 5; ++i) {
                    acc[0][CH * 5 + i] = Y ? mfma(cur[i], xc[0], acc[0][CH * 5 + i]) : mfma(xc[0], cur[i], acc[0][CH * 5 + i]);
                    if constexpr (decltype(FRONT)::value) { if (2 * i < 5) { lo[2 * i] = ldtr(na + 2 * i * 32); hi[2 * i] = ldtr(na + 2 * i * 32 + 3840); } }
                    else { if (decltype(RDX)::value && i == 0) xn[0] = lds128(xa + xoff); lo[i] = ldtr(na + i * 32); }
                    SB;
                    acc[1][CH * 5 + i] = Y ? mfma(cur[i], xc[1], acc[1][CH * 5 + i]) : mfma(xc[1], cur[i], acc[1][CH * 5 + i]);
                    if constexpr (decltype(FRONT)::value) { if (2 * i + 1 < 5) { lo[2 * i + 1] = ldtr(na + (2 * i + 1) * 32); hi[2 * i + 1] = ldtr(na + (2 * i + 1) * 32 + 3840); } }
                    else { hi[i] = ldtr(na + i * 32 + 3840); if (decltype(RDX)::value && i == 0) xn[1] = lds128(xa + xoff + 2048); if (decltype(RDX)::value && i == 4 && !BR) xn[2] = lds128(xa); }
                    SB;
                    if (!BR && halo_owner(CH * 5 + i) == MODE) {
                        const int hi_ = CH * 5 + i - halo_first(MODE & 3);
                        ha[hi_] = Y ? mfma(cur[i], xc[2], ha[hi_]) : mfma(xc[2], cur[i], ha[hi_]);
                        SB;
                    }
                }
#pragma unroll
                for (int i = 0; i < 5; ++i) nxt[i] = cat(lo[i], hi[i]);
            };
            unsigned slot = 2 * STB;
#pragma unroll
            for (int n = 0; n < 15; ++n) asm volatile("" : "+v"(acc[0][n]), "+v"(acc[1][n]));
            LBAR(0); PAD(0);
            STAMP(12); CSTAMP(13);
            xk0[0] = lds128(slot + xh0 + xoff); xk0[1] = lds128(slot + xh0 + xoff + 2048);
#pragma unroll
            for (int i = 0; i < 5; ++i) wA_[i] = cat(ldtr(slot + wlane + i * 32), ldtr(slot + wlane + i * 32 + 3840));
            if (!BR) xk0[2] = lds128(slot + xh0);
            SB;
#pragma unroll 1
            for (int t = 0; t < 10; ++t) {
                const unsigned wa = slot + wlane;
                chunk(IC<0>{}, IC<0>{}, IC<0>{}, wA_, xk0, wB_, wa + 160, xk1, 0u);
                chunk(IC<1>{}, IC<0>{}, IC<0>{}, wB_, xk0, wA_, wa + 320, xk1, 0u);
                chunk(IC<2>{}, IC<0>{}, IC<1>{}, wA_, xk0, wB_, wa + 15360, xk1, slot + xh1);
                chunk(IC<0>{}, IC<0>{}, IC<0>{}, wB_, xk1, wA_, wa + 15360 + 160, xk0, 0u);
                chunk(IC<1>{}, IC<FRONTV>{}, IC<0>{}, wA_, xk1, wB_, wa + 15360 + 320, xk0, 0u);
                slot += STB; if (slot == 3 * STB) slot = 0;
                LBAR(t + 1);
                SB;
                chunk(IC<2>{}, IC<0>{}, IC<1>{}, wB_, xk1, wA_, slot + wlane, xk0, slot + xh0);
            }
            if constexpr (!BR) {
#pragma unroll
                for (int n = 5; n < 15; ++n)
                    if (halo_owner(n) == MODE) {
                        const u2 v = pk4(ha[n - halo_first(MODE & 3)]);
                        sts64(EX_OFF + (n - 5) * 512 + lane * 8, v);
                        sts64(EX_OFF + (10 + n - 5) * 512 + (lane ^ (n < 10 ? 8 : 32)) * 8, v);
                    }
            }
        };
        if (br) mainloop(IC<4>{});
        else if (cw == 0) mainloop(IC<0>{});
        else if (cw == 1) mainloop(IC<1>{});
        else if (cw == 2) mainloop(IC<2>{});
        else mainloop(IC<3>{});
        STAMP(1); CSTAMP(14);
        if (br == 0) {
#pragma unroll
            for (int i = 0; i < 5; ++i) {
                sts64(qa + i * 512, pk4(acc[1][i]));
                sts64(ea + 15360 + i * 512, pk4(acc[1][5 + i]));
                sts64(ea + 15360 + (5 + i) * 512, pk4(acc[1][10 + i]));
            }
#pragma unroll
            for (int i = 0; i < 5; ++i) { ko[i] = pk4(acc[0][5 + i]); vo[i] = pk4(acc[0][10 + i]); }
            if (cw < 3) {
#pragma unroll
                for (int i = 0; i < 5; ++i) { sts64(ea + 10240 + i * 512, ko[i]); sts64(ea + 10240 + (5 + i) * 512, vo[i]); }
            }
            bar();
#pragma unroll
            for (int i = 0; i < 5; ++i) q[i] = pk4(acc[0][i]);
        } else {
#pragma unroll
            for (int i = 0; i < 5; ++i) {
                u2 a2 = pk4(acc[0][10 + i]), b2 = pk4(acc[1][10 + i]);
                u4 t2 = {a2.x, a2.y, b2.x, b2.y};
                sts128(EX_OFF + (cw * 10 + 5 + i) * 1024 + lane * 16, t2);
            }
#pragma unroll
            for (int i = 0; i < 5; ++i) {
                u2 a = pk4(phi4(acc[0][5 + i])), b = pk4(phi4(acc[1][5 + i]));
                u4 t = {a.x, a.y, b.x, b.y};
                sts128(EX_OFF + (cw * 10 + i) * 1024 + lane * 16, t);
            }
            bar();
#pragma unroll
            for (int i = 0; i < 5; ++i) { q[i] = pk4(phi4(acc[0][i])); ko[i] = pk4(phi4(acc[1][i])); }
        }
    }
    STAMP(2); PAD(2);
    const unsigned obase = WS_O + (((br * 16 + rt) * 4 + cw) * 8 + hg) * 5120;
    u2 o[5];
    if (br == 0) {
        const AttnMask mk = attn_mask((rt | cw) != 0, lane);
        u2 kp[5];
        bf8 vf[5];
#pragma unroll
        for (int i = 0; i < 5; ++i) kp[i] = lds64(ea + i * 512);
#pragma unroll
        for (int i = 0; i < 5; ++i) vf[i] = cat(lds64(ea + (5 + i) * 512), vo[i]);
        attn_tile(q, ko, kp, vf, mk, lane, o);
        STAMP(3); STAMP(4);
    } else {
        if (wv >= 3) {
            const u4 ones4 = {0x3F803F80u, 0x3F803F80u, 0x3F803F80u, 0x3F803F80u};
            const bf8 ones = __builtin_bit_cast(bf8, ones4);
            const unsigned pbase = WS_KVP + (hg * 16 + rt) * KVP_STRIDE;
            const int dt = wv == 3 ? 4 : wv - 4;
            f4 t[3] = {z4, z4, z4}, tz = z4;
            const int e0 = max(dt - 1, 0), e2 = min(dt + 1, 4);
#pragma unroll
            for (int s = 0; s < 4; ++s) {
                bf8 kf = lds128(EX_OFF + (s * 10 + dt) * 1024 + lane * 16);
                t[0] = mfma(kf, lds128(EX_OFF + (s * 10 + 5 + e0) * 1024 + lane * 16), t[0]);
                t[1] = mfma(kf, lds128(EX_OFF + (s * 10 + 5 + dt) * 1024 + lane * 16), t[1]);
                t[2] = mfma(kf, lds128(EX_OFF + (s * 10 + 5 + e2) * 1024 + lane * 16), t[2]);
                tz = mfma(kf, ones, tz);
            }
            const int Q = 4 * dt + g;
#pragma unroll
            for (int a = 0; a < 3; ++a) {
                int et = dt - 1 + a;
                int e = 16 * et + c, h = (e * 205) >> 12, u = Q - 5 * h;
                unsigned off = ((unsigned)et <= 4u && (unsigned)u <= 4u) ? pbase + (u * 80 + e) * 16 : 0xF0000000u;
                __builtin_amdgcn_raw_buffer_store_b128(__builtin_bit_cast(u4, t[a]), rs, off, 0, 16);
            }
            __builtin_amdgcn_raw_buffer_store_b128(__builtin_bit_cast(u4, tz), rs, c == 0 ? pbase + (400 + Q) * 16 : 0xF0000000u, 0, 16);
        }
        {
            const u4 zz = {0u, 0u, 0u, 0u};
            sts128(IMG_OFF + tid * 16, zz); sts128(IMG_OFF + 8192 + tid * 16, zz);
        }
        wait_vm<0>();
        bar();
        if (wv == 4) {
            if (lane == 0) st_agent(ws + WS_FKV + (hg * 16 + rt) * 128, tag);
            poll_all(ws + WS_FKV + (hg * 16 + c) * 128, tag);
        }
        if (wv < 4) {
#pragma unroll
            for (int i = 0; i < 5; ++i) sts64(PQ_OFF + (cw * 5 + i) * 512 + lane * 8, ko[i]);
        }
        bar();
        STAMP(3);
        if (tid < 420) {
            f4 s = z4;
            u4 v[16];
#pragma unroll
            for (int r = 0; r < 16; ++r) v[r] = __builtin_amdgcn_raw_buffer_load_b128(rs, tid * 16, WS_KVP + (hg * 16 + ((r + 2 * rt) & 15)) * KVP_STRIDE, 16);
#pragma unroll
            for (int r = 0; r < 16; ++r) s += __builtin_bit_cast(f4, v[r]);
            int u = tid / 80, e = tid - u * 80, h = e / 20;
            unsigned a = tid < 400 ? e * 176 + (5 * h + u) * 8 : (80 + (tid - 400) / 5) * 176 + (tid - 400) * 8;
            sts64(IMG_OFF + a, pk4(s));
        }
        if (wv >= 4) {
#pragma unroll
            for (int i = 0; i < 5; ++i) q[i] = lds64(PQ_OFF + (cw * 5 + i) * 512 + lane * 8);
        }
        bar();
        STAMP(4);
        const unsigned ia = IMG_OFF + c * 176 + g * 8;
        const unsigned za = IMG_OFF + (80 + (c & 3)) * 176 + g * 8;
        bf8 zf0 = cat(lds64(za), lds64(za + 32)), zf1 = cat(lds64(za + 64), lds64(za + 96)), zf2 = cat(lds64(za + 128), zero2);
        bf8 kf[9];
        kf[0] = cat(lds64(ia), lds64(ia + 32));
        kf[1] = cat(lds64(ia + 2816), lds64(ia + 2816 + 32));
        kf[2] = cat(lds64(ia + 2816 + 64), lds64(ia + 2816 + 96));
        kf[3] = cat(lds64(ia + 2 * 2816), lds64(ia + 2 * 2816 + 32));
        kf[4] = cat(lds64(ia + 2 * 2816 + 64), lds64(ia + 2 * 2816 + 96));
        kf[5] = cat(lds64(ia + 3 * 2816 + 64), lds64(ia + 3 * 2816 + 96));
        kf[6] = cat(lds64(ia + 3 * 2816 + 128), zero2);
        kf[7] = cat(lds64(ia + 4 * 2816 + 64), lds64(ia + 4 * 2816 + 96));
        kf[8] = cat(lds64(ia + 4 * 2816 + 128), zero2);
        bf8 p01 = cat(q[0], q[1]), p23 = cat(q[2], q[3]), p4 = cat(q[4], zero2);
        f4 den = mfma(zf2, p4, mfma(zf1, p23, mfma(zf0, p01, z4)));
        f4 n0 = mfma(kf[0], p01, z4);
        f4 n1 = mfma(kf[2], p23, mfma(kf[1], p01, z4));
        f4 n2 = mfma(kf[4], p23, mfma(kf[3], p01, z4));
        f4 n3 = mfma(kf[6], p4, mfma(kf[5], p23, z4));
        f4 n4 = mfma(kf[8], p4, mfma(kf[7], p23, z4));
        float r0 = __builtin_amdgcn_rcpf(den[0] + 1e-6f), r1 = __builtin_amdgcn_rcpf(den[1] + 1e-6f);
        float r2 = __builtin_amdgcn_rcpf(den[2] + 1e-6f), r3 = __builtin_amdgcn_rcpf(den[3] + 1e-6f);
        o[0] = pk4(n0 * r0);
        o[1] = pk4(n1 * (g < 1 ? r0 : r1));
        o[2] = pk4(n2 * (g < 2 ? r1 : r2));
        o[3] = pk4(n3 * (g < 3 ? r2 : r3));
        o[4] = pk4(n4 * r3);
    }
    store_o(rs, obase, par, lane, o);
    wait_vm<0>();
    STAMP(11);
    bar();
    if (tid == 0) st_agent(ws + WS_FO + ((br * 16 + rt) * 8 + hg) * 128, tag);
    STAMP(5);

    {
        const unsigned wl = WO_OFF + (lane >> 2) * 160 + (lane & 3) * 8;
        unsigned gb[4], so[4];
        const unsigned ob = WS_O + ((br * 16 + rt2) * 4 + cw) * 8 * 5120;
#pragma unroll
        for (int gg = 0; gg < 4; ++gg) { const int gr = (gg + ct + 4 * par) & 7; gb[gg] = wl + gr * 12800; so[gg] = ob + gr * 5120; }
        bf8 wA_[5], wB_[5];
#pragma unroll
        for (int i = 0; i < 5; ++i) wA_[i] = cat(ldtr(gb[0] + i * 32), ldtr(gb[0] + i * 32 + 16 * 160));
        f4 a2[2][5];
#pragma unroll
        for (int n = 0; n < 5; ++n) { a2[0][n] = z4; a2[1][n] = z4; asm volatile("" : "+v"(a2[0][n]), "+v"(a2[1][n])); }
        poll_all(ws + WS_FO + ((br * 16 + rt2) * 8 + ((ct + 4 * par + (lane & 3)) & 7)) * 128, tag);
        STAMP(6); PAD(6);
        u4 of[4][4];
        u2 ot[4][2];
        auto ld = [&](auto SC, int half) {
            constexpr int s = decltype(SC)::value;
            if constexpr (s < 10) {
                constexpr int u = s / 5, r = s % 5;
                if constexpr (r < 4) {
                    constexpr int gg = 2 * u + (r >> 1), pr = r & 1;
                    of[gg][2 * half + pr] = __builtin_amdgcn_raw_buffer_load_b128(rs, half * 2048 + pr * 1024 + lane * 16, so[gg], 16);
                } else {
                    ot[2 * u][half] = __builtin_amdgcn_raw_buffer_load_b64(rs, half * 512 + lane * 8, so[2 * u] + 4096, 16);
                    ot[2 * u + 1][half] = __builtin_amdgcn_raw_buffer_load_b64(rs, half * 512 + lane * 8, so[2 * u + 1] + 4096, 16);
                }
            }
        };
#define P2AHEAD 3
        ld(IC<0>{}, 0); ld(IC<0>{}, 1); ld(IC<1>{}, 0); ld(IC<1>{}, 1);
        if (P2AHEAD > 2) { ld(IC<2>{}, 0); ld(IC<2>{}, 1); }
        if (P2AHEAD > 3) { ld(IC<3>{}, 0); ld(IC<3>{}, 1); }
        SB;
#define B8(x) __builtin_bit_cast(bf8, x)
        auto step = [&](auto SC, const bf8 (&cur)[5], bf8 (&nxt)[5]) {
            constexpr int s = decltype(SC)::value;
            constexpr int u = s / 5, r = s % 5;
            bf8 o0, o1;
            if constexpr (r < 4) { o0 = B8(of[2 * u + (r >> 1)][r & 1]); o1 = B8(of[2 * u + (r >> 1)][2 + (r & 1)]); }
            else { o0 = cat(ot[2 * u][0], ot[2 * u + 1][0]); o1 = cat(ot[2 * u][1], ot[2 * u + 1][1]); }
            constexpr int s1 = s + 1, u1 = (s1 / 5) & 1, r1 = s1 % 5;
            const unsigned na = r1 < 4 ? gb[2 * u1 + ((r1 >> 1) & 1)] + (r1 & 1) * 32 * 160 : gb[2 * u1] + 64 * 160;
            const unsigned nb = r1 < 4 ? na + 16 * 160 : gb[2 * u1 + 1] + 64 * 160;
            bf4 lo[5], hi[5];
#pragma unroll
            for (int i = 0; i < 5; ++i) {
                a2[0][i] = mfma(cur[i], o0, a2[0][i]); if (s < 9) lo[i] = ldtr(na + i * 32);
                if (i == 1) ld(IC<s + P2AHEAD>{}, 0);
                if (i == 3) ld(IC<s + P2AHEAD>{}, 1);
                SB;
                a2[1][i] = mfma(cur[i], o1, a2[1][i]); if (s < 9) hi[i] = ldtr(nb + i * 32); SB;
            }
            if (s < 9) {
#pragma unroll
                for (int i = 0; i < 5; ++i) nxt[i] = cat(lo[i], hi[i]);
            }
        };
        step(IC<0>{}, wA_, wB_); step(IC<1>{}, wB_, wA_); step(IC<2>{}, wA_, wB_); step(IC<3>{}, wB_, wA_); step(IC<4>{}, wA_, wB_);
        step(IC<5>{}, wB_, wA_); step(IC<6>{}, wA_, wB_); step(IC<7>{}, wB_, wA_); step(IC<8>{}, wA_, wB_); step(IC<9>{}, wB_, wA_);
        STAMP(7);
#pragma unroll
        for (int n = 0; n < 5; ++n) sts128(EX_OFF + (wv * 5 + n) * 1024 + lane * 16, __builtin_bit_cast(u4, par ? a2[0][n] : a2[1][n]));
        bar();
        char* op = out + (size_t)(rt2 * 128 + 32 * cw + 2 * c + par) * 2560 + br * 1280 + ct * 160 + g * 8;
#pragma unroll
        for (int n = 0; n < 5; ++n) {
            const f4 other = *(LAS f4*)(size_t)(EX_OFF + ((wv ^ 4) * 5 + n) * 1024 + lane * 16);
            const f4 mine = par ? a2[1][n] : a2[0][n];
            *(GAS u2*)(op + n * 32) = pk4(par ? other + mine : mine + other);
        }
        STAMP(8); PAD(8);
        LDUMP(); SDUMP();
    }
    if (bid == 0 && wv == 7) {
        const char* p = ws + WS_FO + lane * 128;
#pragma unroll 1
        for (unsigned spins = 0; spins < (1u << 18); ++spins) {
            const u64 v0 = ld_agent(p), v1 = ld_agent(p + 8192), v2 = ld_agent(p + 16384), v3 = ld_agent(p + 24576);
            if (__all((v0 == tag) & (v1 == tag) & (v2 == tag) & (v3 == tag))) break;
            __builtin_amdgcn_s_sleep(2);
        }
        if (lane == 0) st_agent(ws + WS_EPOCH, tag);
    }
}

extern "C" void kernel_launch(void* const* d_in, const int* in_sizes, int n_in, void* d_out, int out_size, void* d_ws, size_t ws_size,
                              hipStream_t stream) {
    const char *xA = (const char*)d_in[0], *wA = (const char*)d_in[1], *woA = (const char*)d_in[2];
    const char *xB = (const char*)d_in[3], *wB = (const char*)d_in[4], *woB = (const char*)d_in[5];
    (void)hipFuncSetAttribute((const void*)fused_kernel, hipFuncAttributeMaxDynamicSharedMemorySize, LDS_TOTAL);
    fused_kernel<<<256, 512, LDS_TOTAL, stream>>>(xA, wA, woA, xB, wB, woB, (char*)d_out, (char*)d_ws);
}
```

```cpp
#include <hip/hip_runtime.h>
#include <hip/hip_bf16.h>

typedef __bf16 bf8 __attribute__((ext_vector_type(8)));
typedef __bf16 bf4 __attribute__((ext_vector_type(4)));
typedef __bf16 bf2 __attribute__((ext_vector_type(2)));
typedef float f4 __attribute__((ext_vector_type(4)));
typedef float f2 __attribute__((ext_vector_type(2)));
typedef unsigned u4 __attribute__((ext_vector_type(4)));
typedef unsigned u2 __attribute__((ext_vector_type(2)));
typedef unsigned long long u64;
#define GAS __attribute__((address_space(1)))
#define LAS __attribute__((address_space(3)))
#define DEV __device__ __forceinline__
template <int V> struct IC { static constexpr int value = V; };

constexpr unsigned WS_EPOCH = 0;
constexpr unsigned WS_FKV = 4096;
constexpr unsigned WS_FO = WS_FKV + 128 * 128;
constexpr unsigned WS_KVP = 65536;
constexpr unsigned KVP_STRIDE = 3456;
constexpr unsigned WS_O = 1u << 20;
constexpr unsigned WS_TOTAL = 7u << 20;

constexpr unsigned LDS_TOTAL = 163840;
constexpr unsigned wo_pair(int br, int u) { return br ? (u == 0 ? 112640u : u == 1 ? 138240u : u == 2 ? 51200u : 76800u) : (u == 0 ? 0u : u == 1 ? 138240u : u == 2 ? 112640u : 25600u); }
constexpr unsigned OT_A = 61440, OT_B = 16384;
constexpr unsigned XB = 144 * 128, WB = 64 * 480, STB = XB + WB;
constexpr unsigned EX_OFF = 26624, EXQ_OFF = EX_OFF + 51200;
constexpr unsigned DUMMY_A = 148480;
constexpr unsigned XBB = 128 * 128, KVB = 64 * 352, STBB = XBB + KVB;
constexpr unsigned RB0 = LDS_TOTAL - 3 * STBB;
constexpr unsigned EXB_OFF = LDS_TOTAL - 40960;
constexpr unsigned CNT_OFF = 32;
constexpr unsigned WQ_OFF = 256;
constexpr unsigned IMG_OFF = WQ_OFF + 102400;
constexpr unsigned PQ_OFF = WQ_OFF;

DEV void glds16(const char* base, unsigned off, unsigned lds) {
    asm volatile("s_mov_b32 m0, %2\n\ts_nop 0\n\tglobal_load_lds_dwordx4 %0, %1" ::"v"(off), "s"(base), "s"(__builtin_amdgcn_readfirstlane(lds)) : "memory");
}
DEV void glds16m(const char* base, unsigned off, unsigned lds, u64 mask) {
    asm volatile("s_mov_b32 m0, %2\n\ts_mov_b64 exec, %3\n\tglobal_load_lds_dwordx4 %0, %1\n\ts_mov_b64 exec, -1" ::"v"(off), "s"(base), "s"(__builtin_amdgcn_readfirstlane(lds)), "s"(mask) : "memory");
}
DEV void touch4m(const char* base, unsigned off, unsigned lds, u64 mask) {
    asm volatile("s_mov_b32 m0, %2\n\ts_mov_b64 exec, %3\n\tglobal_load_lds_dword %0, %1\n\ts_mov_b64 exec, -1" ::"v"(off), "s"(base), "s"(__builtin_amdgcn_readfirstlane(lds)), "s"(mask) : "memory");
}
#define SB __builtin_amdgcn_sched_barrier(0)
template <int N> DEV void wait_vm() { asm volatile("s_waitcnt vmcnt(%0)" ::"i"(N) : "memory"); }
DEV void bar() { asm volatile("" ::: "memory"); __builtin_amdgcn_s_waitcnt(0xC07F); asm volatile("s_barrier" ::: "memory"); }
DEV bf8 lds128(unsigned a) { return *(LAS bf8*)(size_t)a; }
DEV u2 lds64(unsigned a) { return *(LAS u2*)(size_t)a; }
DEV void sts64(unsigned a, u2 v) { *(LAS u2*)(size_t)a = v; }
DEV void sts128(unsigned a, u4 v) { *(LAS u4*)(size_t)a = v; }
#define lds2x64(a, o0, o1) ({ bf8 r_; asm volatile("ds_read2st64_b64 %0, %1 offset0:%2 offset1:%3" : "=v"(r_) : "v"(a), "i"(o0), "i"(o1) : "memory"); r_; })
DEV bf4 ldtr(unsigned a) { return __builtin_amdgcn_ds_read_tr16_b64_v4bf16((LAS bf4*)(size_t)a); }
DEV bf8 cat(bf4 a, bf4 b) { return __builtin_shufflevector(a, b, 0, 1, 2, 3, 4, 5, 6, 7); }
DEV bf8 cat(u2 a, u2 b) { u4 r = {a.x, a.y, b.x, b.y}; return __builtin_bit_cast(bf8, r); }
DEV f4 mfma(bf8 a, bf8 b, f4 c) { return __builtin_amdgcn_mfma_f32_16x16x32_bf16(a, b, c, 0, 0, 0); }
DEV unsigned pk2(float a, float b) { f2 t = {a, b}; return __builtin_bit_cast(unsigned, __builtin_convertvector(t, bf2)); }
DEV u2 pk4(f4 v) { u2 r = {pk2(v[0], v[1]), pk2(v[2], v[3])}; return r; }
DEV float fmx(float a, float b) { return __builtin_elementwise_maximum(a, b); }
DEV float phi(float a) {
    float e = __builtin_amdgcn_fmed3f(__builtin_amdgcn_exp2f(__builtin_fmaf(a, 1.4426950408889634f, -1.4426950408889634f)), 0.f, 1.f);
    return fmx(a, e) + 1e-6f;
}
DEV f4 phi4(f4 v) { f4 r = {phi(v[0]), phi(v[1]), phi(v[2]), phi(v[3])}; return r; }
DEV u64 mix64(u64 z) {
    z += 0x9E3779B97F4A7C15ull;
    z = (z ^ (z >> 30)) * 0xBF58476D1CE4E5B9ull;
    z = (z ^ (z >> 27)) * 0x94D049BB133111EBull;
    return z ^ (z >> 31);
}
DEV u64 ld_agent(const char* p) { return __hip_atomic_load((GAS u64*)p, __ATOMIC_RELAXED, __HIP_MEMORY_SCOPE_AGENT); }
DEV void st_agent(char* p, u64 v) { __hip_atomic_store((GAS u64*)p, v, __ATOMIC_RELAXED, __HIP_MEMORY_SCOPE_AGENT); }
DEV void poll_all(const char* p, u64 tag) {
#pragma unroll 1
    for (unsigned spins = 0; spins < (1u << 18); ++spins) {
        u64 v = ld_agent(p);
        if (__all(v == tag)) break;
        __builtin_amdgcn_s_sleep(1);
    }
    __builtin_amdgcn_fence(__ATOMIC_ACQUIRE, "wavefront");
}
DEV void cnt_add(unsigned a) {
    asm volatile("" ::: "memory"); __builtin_amdgcn_s_waitcnt(0xC07F); asm volatile("" ::: "memory");
    if ((threadIdx.x & 63) == 0) __hip_atomic_fetch_add((LAS unsigned*)(size_t)a, 1u, __ATOMIC_RELAXED, __HIP_MEMORY_SCOPE_WORKGROUP);
}
DEV void cnt_wait(unsigned a, unsigned n) {
#pragma unroll 1
    for (int spins = 0; spins < (1 << 22); ++spins)
        if ((unsigned)__builtin_amdgcn_readfirstlane(__hip_atomic_load((LAS unsigned*)(size_t)a, __ATOMIC_RELAXED, __HIP_MEMORY_SCOPE_WORKGROUP)) >= n) break;
    asm volatile("" ::: "memory");
}
DEV unsigned fb(float x) { return __builtin_bit_cast(unsigned, x); }
DEV float bf(unsigned x) { return __builtin_bit_cast(float, x); }
DEV unsigned opaque(unsigned x) { asm volatile("" : "+v"(x)); return x; }
DEV void rowmax4x4(float (&m)[4]) {
    auto a = __builtin_amdgcn_permlane16_swap(fb(m[0]), fb(m[1]), false, false);
    auto b = __builtin_amdgcn_permlane16_swap(fb(m[2]), fb(m[3]), false, false);
    float u = fmx(bf(a[0]), bf(a[1])), w = fmx(bf(b[0]), bf(b[1]));
    auto d = __builtin_amdgcn_permlane32_swap(fb(u), fb(w), false, false);
    unsigned t = fb(fmx(bf(d[0]), bf(d[1])));
    auto e = __builtin_amdgcn_permlane32_swap(t, opaque(t), false, false);
    auto f = __builtin_amdgcn_permlane16_swap(e[0], opaque(e[0]), false, false);
    auto h = __builtin_amdgcn_permlane16_swap(e[1], opaque(e[1]), false, false);
    m[0] = bf(f[0]); m[1] = bf(f[1]); m[2] = bf(h[0]); m[3] = bf(h[1]);
}

DEV void store_o(__amdgpu_buffer_rsrc_t rs, unsigned obase, int par, int lane, const u2 (&t)[5]) {
    u4 a = {t[0].x, t[0].y, t[1].x, t[1].y}, b = {t[2].x, t[2].y, t[3].x, t[3].y};
    __builtin_amdgcn_raw_buffer_store_b128(a, rs, lane * 16, obase + par * 2048, 16);
    __builtin_amdgcn_raw_buffer_store_b128(b, rs, 1024 + lane * 16, obase + par * 2048, 16);
    __builtin_amdgcn_raw_buffer_store_b64(t[4], rs, lane * 8, obase + 4096 + par * 512, 16);
}

struct AttnMask { f4 co, cp; unsigned mk01, mk23; unsigned qlo[4], qhi[4]; };
DEV AttnMask attn_mask(bool prev_ok, int lane) {
    const int c = lane & 15, g = lane >> 4;
    AttnMask k;
    const float ninf = -__builtin_inff();
#pragma unroll
    for (int r = 0; r < 4; ++r) {
        const int d = c - (4 * g + r);
        k.co[r] = (unsigned)d <= 7u ? 0.f : ninf;
        k.cp[r] = (prev_ok & (d <= -9)) ? 0.f : ninf;
    }
    k.mk01 = (4 * g <= c ? 0xFFFFu : 0u) | (4 * g + 1 <= c ? 0xFFFF0000u : 0u);
    k.mk23 = (4 * g + 2 <= c ? 0xFFFFu : 0u) | (4 * g + 3 <= c ? 0xFFFF0000u : 0u);
#pragma unroll
    for (int h = 0; h < 4; ++h) { k.qlo[h] = g >= h ? ~0u : 0u; k.qhi[h] = g <= h ? ~0u : 0u; }
    return k;
}
DEV void attn_tile(const u2 (&q)[5], const bf8 (&kfo)[4], const bf8 (&kfp)[4], const bf8 (&vf)[5], const AttnMask& k, int lane, u2 (&out)[5]) {
    const int g = lane >> 4;
    const float sc = 0.22360679774997896f * 1.4426950408889634f;
    const u4 ones4 = {0x3F803F80u, 0x3F803F80u, 0x3F803F80u, 0x3F803F80u};
    const bf8 ones = __builtin_bit_cast(bf8, ones4);
    const f4 z4 = {0.f, 0.f, 0.f, 0.f};
    f4 so[4], sp[4], s[4];
    float m[4], rs[4];
    bf8 P[4];
#pragma unroll
    for (int h = 0; h < 4; ++h) {
        u2 a = q[h], b = q[h + 1];
        if (h > 0) { a.x &= k.qlo[h]; a.y &= k.qlo[h]; }
        if (h < 3) { b.x &= k.qhi[h]; b.y &= k.qhi[h]; }
        bf8 qf = cat(a, b);
        so[h] = mfma(kfo[h], qf, k.co);
        sp[h] = mfma(kfp[h], qf, k.cp);
    }
#pragma unroll
    for (int h = 0; h < 4; ++h) {
        s[h] = __builtin_elementwise_maximum(so[h], sp[h]);
        m[h] = fmx(fmx(s[h][0], s[h][1]), fmx(s[h][2], s[h][3]));
    }
    rowmax4x4(m);
#pragma unroll
    for (int h = 0; h < 4; ++h) {
        float p[4];
        const float ms = m[h] * sc;
#pragma unroll
        for (int r = 0; r < 4; ++r) p[r] = __builtin_amdgcn_exp2f(__builtin_fmaf(s[h][r], sc, -ms));
        const unsigned p01 = pk2(p[0], p[1]), p23 = pk2(p[2], p[3]);
        u2 pp = {p01 & ~k.mk01, p23 & ~k.mk23};
        u2 po = {p01 & k.mk01, p23 & k.mk23};
        P[h] = cat(pp, po);
    }
    f4 sm[4], oa[5], ob[3];
#pragma unroll
    for (int h = 0; h < 4; ++h) sm[h] = mfma(ones, P[h], z4);
    oa[0] = mfma(vf[0], P[0], z4);
#pragma unroll
    for (int i = 1; i < 4; ++i) { oa[i] = mfma(vf[i], P[i - 1], z4); ob[i - 1] = mfma(vf[i], P[i], z4); }
    oa[4] = mfma(vf[4], P[3], z4);
#pragma unroll
    for (int h = 0; h < 4; ++h) rs[h] = __builtin_amdgcn_rcpf(sm[h][0]);
    out[0] = pk4(oa[0] * rs[0]);
#pragma unroll
    for (int i = 1; i < 4; ++i) {
        const bool lo = g < i;
        const f4 o = lo ? oa[i] : ob[i - 1];
        out[i] = pk4(o * (lo ? rs[i - 1] : rs[i]));
    }
    out[4] = pk4(oa[4] * rs[3]);
}

constexpr int NH = 5, NHB = 4;
constexpr int BACKA = 20, BACKK = 16;
constexpr int BWT = 4, BLF = 6;
constexpr int QB = 2, QP = 7, QU = 5;
constexpr int WOA0 = 6, WOB1 = 12;
constexpr int P2AHEAD = 7, PRE2A = 4, PRE2 = 9;

template <int N, class F> DEV void static_for(F&& f) {
    if constexpr (N > 0) { static_for<N - 1>(f); f(IC<N - 1>{}); }
}
#define B8(x) __builtin_bit_cast(bf8, x)

constexpr int a_nt(int mode) { return (mode == 1) ? 7 : 8; }
constexpr int a_tile(int mode, int i) { return !(mode & 1) ? i : (mode == 1 ? 8 + i : (i < 3 ? 8 + i : (i == 3 ? 7 : 7 + i))); }
constexpr bool a_main(int mode, int n) { return (mode & 1) ? n >= 8 : true; }
constexpr bool a_halo(int mode, int n) { return mode == 0 ? n == 5 : mode == 2 ? n == 6 : mode == 1 ? (n >= 8 && n <= 11) : (n == 7 || n >= 12); }
constexpr int a_hidx(int mode, int n) { return mode == 1 ? n - 8 : mode == 3 ? (n == 7 ? 3 : n - 12) : 0; }

struct SPlan {
    int ns, nts, bpos, nwrap;
    int ts[72], m[72];
    int at[72];
    int wrap[32];
};
constexpr SPlan make_plan(int nts, const int* nmain, const int* halo, int nx, int back, int gap) {
    SPlan p{};
    p.nts = nts;
    int rd[64] = {}, nr = 0, q = 0;
    bool seen[2][8] = {};
    for (int t = 0; t < nts; ++t) {
        const int kk = t >= nts / 2;
        rd[nr++] = 2 * t; rd[nr++] = 2 * t + 1;
        for (int i = 0; i < nmain[t] + halo[t]; ++i) {
            const int m = i < nmain[t] ? i : 4;
            if (!seen[kk][m]) { seen[kk][m] = true; rd[nr++] = 64 + 8 * kk + m; }
            p.ts[q] = t; p.m[q] = m; ++q;
        }
    }
    p.ns = q; p.bpos = q - back;
    for (int i = 0; i < q; ++i) p.at[i] = -1;
    for (int k = 0; k < nr; ++k) {
        const int pos = k * (q - gap) / nr;
        p.at[(p.bpos + pos) % q] = rd[k];
        if (pos < back) p.wrap[p.nwrap++] = rd[k];
    }
    return p;
}
constexpr SPlan a_plan(int mode) {
    int nm[16] = {}, hl[16] = {};
    for (int t = 0; t < 2 * a_nt(mode); ++t) { const int n = a_tile(mode, t % a_nt(mode)); nm[t] = a_main(mode, n) ? 4 : 0; hl[t] = a_halo(mode, n); }
    return make_plan(2 * a_nt(mode), nm, hl, 5, BACKA, 4);
}
constexpr int plan_wts(const SPlan& p) { int n = 0; for (int i = 0; i < p.nwrap; ++i) if (p.wrap[i] < 64) ++n; return n; }
constexpr bool plan_wrapped(const SPlan& p, int r) { for (int i = 0; i < p.nwrap; ++i) if (p.wrap[i] == r) return true; return false; }

constexpr int al_h(int mode) { return (plan_wts(a_plan(mode)) + 1) / 2; }
constexpr int al_tile(int mode, int u) { const int H = al_h(mode); return u < 2 * H ? u % H : u / 2; }
constexpr int al_kk(int mode, int u) { const int H = al_h(mode); return u < 2 * H ? u / H : u & 1; }
constexpr int al_start(int mode, int u) { int q = 0; for (int v = 0; v < u; ++v) { const int n = a_tile(mode, al_tile(mode, v)); q += (a_main(mode, n) ? 4 : 0) + (a_halo(mode, n) ? 1 : 0); } return q; }
struct ALPlan { int act[72]; int rest; };
constexpr bool al_has(int mode, int p) { const int n = a_tile(mode, p / 5); return p % 5 < 4 ? a_main(mode, n) : a_halo(mode, n); }
constexpr ALPlan al_plan(int mode) {
    ALPlan pl{};
    const SPlan sp = a_plan(mode);
    const int NT = a_nt(mode);
    int fin[8] = {}, rd[64] = {}, dl[64] = {}, nr = 0, q = 0;
    bool seen[2][8] = {};
    for (int u = 0; u < 2 * NT; ++u) {
        const int i = al_tile(mode, u), kk = al_kk(mode, u), n = a_tile(mode, i), ts = kk * NT + i;
        for (int h = 0; h < 2; ++h) if (!plan_wrapped(sp, 2 * ts + h)) { rd[nr] = 2 * ts + h; dl[nr++] = q; }
        for (int m = 0; m < 5; ++m)
            if (m < 4 ? a_main(mode, n) : a_halo(mode, n)) {
                if (!seen[kk][m]) { seen[kk][m] = true; if (!plan_wrapped(sp, 64 + 8 * kk + m)) { rd[nr] = 64 + 8 * kk + m; dl[nr++] = q; } }
                ++q;
            }
        if (kk) fin[i] = q;
    }
    int r = 0, p = 0;
    for (int i = 0; i < q; ++i) {
        while (p < 5 * NT && !al_has(mode, p)) ++p;
        const int d = r >= nr ? 1000 : dl[r];
        const bool putok = p < 5 * NT && i >= fin[p / 5] + 1;
        if (r < nr && (d - i <= 10 || !putok) && d - i <= 24) pl.act[i] = rd[r++];
        else if (putok) pl.act[i] = 100 + p++;
        else pl.act[i] = -1;
    }
    pl.rest = p;
    return pl;
}

constexpr int b_nm(int p) { return p % 3 == 2 ? 2 : 4; }
constexpr int b_cum(int p) { return (p / 3) * 10 + (p % 3) * 4; }
constexpr SPlan b_plan(int back) {
    SPlan p{};
    const int half = 6;
    int rd[64] = {}, nr = 0, q = 0;
    for (int kk = 0; kk < 2; ++kk)
        for (int n = 0; n < half; ++n) {
            const int f = 9 * kk + n;
            rd[nr++] = 2 * f; rd[nr++] = 2 * f + 1;
            for (int m = 0; m < b_nm(n); ++m) { if (n == 0) rd[nr++] = 64 + 8 * kk + m; p.ts[q] = f; p.m[q] = m; ++q; }
        }
    p.ns = q; p.bpos = q - back; p.nwrap = 4 + 2 * BWT;
    for (int i = 0; i < q; ++i) p.at[i] = -1;
    for (int k = 0; k < p.nwrap; ++k) { p.at[p.bpos + k * back / p.nwrap] = rd[k]; p.wrap[k] = rd[k]; }
    for (int k = p.nwrap; k < nr; ++k) p.at[(k - p.nwrap) * (p.bpos - 4) / (nr - p.nwrap)] = rd[k];
    return p;
}
constexpr int bl_f(int p) { return 9 * ((p / 3) & 1) + (p % 3) + 3 * (p / 6); }
constexpr int bl_act(int q) {
    int k = 0;
    for (int p = 0; p < 12; ++p) {
        const int f = bl_f(p);
        for (int h = 0; h < 2; ++h) if (f >= BWT) { if (k == q) return 2 * f + h; ++k; }
        if (p == 3) for (int m = 0; m < 4; ++m) { if (k == q) return 72 + m; ++k; }
    }
    return -1;
}

__global__ __launch_bounds__(512, 2) void fused_kernel(const char* __restrict__ xA, const char* __restrict__ wA, const char* __restrict__ xB,
                                                       const char* __restrict__ wB, const char* __restrict__ woA, const char* __restrict__ woB,
                                                       char* __restrict__ out, char* __restrict__ ws) {
    extern __shared__ __attribute__((aligned(16))) char smem[];
    const int bid = blockIdx.x, xcd = bid & 7, j = bid >> 3;
    const int br = xcd & 1, xq = xcd >> 1;
    const int rt = (xq >> 1) * 8 + (j & 7), hg = (xq & 1) * 4 + (j >> 3);
    const int rt2 = xq * 4 + (j & 3), ct = j >> 2;
    const int tid = threadIdx.x, lane = tid & 63;
    const int wv = __builtin_amdgcn_readfirstlane(tid >> 6);
    const int cw = wv & 3, par = wv >> 2;
    const int c = lane & 15, g = lane >> 4;
    const f4 z4 = {0.f, 0.f, 0.f, 0.f};
    const f4 one4 = {1.f, 1.f, 1.f, 1.f};
    typedef u64 u64x2 __attribute__((ext_vector_type(2)));
    u64x2 xw;
    asm volatile("s_load_dwordx4 %0, %1, %2" : "=s"(xw) : "s"(__builtin_amdgcn_kernarg_segment_ptr()), "s"(br * 16));
    int ph = ((5 * (j & 7)) >> 2) + ((5 * (j >> 3)) >> 1); ph = ph >= 10 ? ph - 10 : ph;
    const int cl = ((j >> 3) - j) & 3;
    const u64 rot = (0x9876543210ull >> (4 * ph)) | (0x9876543210ull << (4 * (10 - ph)));
    const unsigned clo = (unsigned)(((cl & 2) ? 0x6547210375416032ull : 0x6940352165832140ull) >> (32 * (cl & 1))), chi = (0x98988797u >> (8 * cl)) & 255u;
    const u64 seq = br ? rot : (((u64)chi << 32) | clo);
    const unsigned voffx = (rt * 128 + (cw >> 1) + 16 * (cw & 1) + 2 * (lane >> 3)) * 1280 + (((lane & 7) ^ (4 * (cw & 1) + (lane >> 4))) & 7) * 16;
    { unsigned vx = voffx; u64 sq = seq; asm volatile("s_waitcnt lgkmcnt(0)" : "+s"(xw) : "v"(vx), "s"(sq)); }
    const char* x = (const char*)xw[0];
    const char* w = (const char*)xw[1];
    auto ldx = [&](int t, unsigned dst) {
        const char* xb = x + ((int)(seq >> (4 * t)) & 15) * 128;
#pragma unroll
        for (int i = 0; i < 4; ++i) glds16(xb + i * 40960, voffx, dst + cw * 1024 + i * 4096);
    };
    if (__builtin_expect(par, 1)) {
        __builtin_amdgcn_s_setprio(3);
        ldx(0, br ? RB0 + 2 * STBB : 2 * STB + 2048);
    }
    else if (wv == 0 && j < 8) {
        const char* pc; unsigned len;
        asm volatile("s_getpc_b64 %0\n1:\n\ts_mov_b32 %1, fk_tail_label-1b" : "=s"(pc), "=s"(len));
        touch4m(pc, min((unsigned)(j * 64 + lane) * 128u, len - 4u), br ? 1024u : DUMMY_A, ~0ull);
    }
    SB;
    constexpr u64 M60 = (1ull << 60) - 1;
    const int r10 = (lane * 26) >> 8;
    const unsigned l10 = (lane - 10 * r10) * 16 + hg * 160;
    const char* wo = br ? woB : woA;
    auto wo_load = [&](auto I0, auto NC, auto STEP, int dyn) {
        constexpr int i0 = decltype(I0)::value, n = decltype(NC)::value, istep = decltype(STEP)::value;
        const unsigned l_ = opaque(lane);
        const unsigned voffo = ((l_ * 26) >> 8) * 1120 + l_ * 16 + ct * 160 + cw * 6400 + dyn * 25600;
        const unsigned d0 = cw * 800 + dyn * 3200;
#pragma unroll
        for (int k = 0; k < n; ++k) {
            const int i = i0 + k * istep;
            glds16m(wo + ((((i >> 2) + ct) & 7) * 4 + (i & 3)) * 25600, voffo, (br ? wo_pair(1, i >> 3) : wo_pair(0, i >> 3)) + (i & 7) * 3200 + d0, (1ull << 50) - 1);
        }
    };
    auto wo_touch = [&](unsigned dst) { touch4m(wo + (j * 200 + cw * 50) * 128, lane * 128, dst, (1ull << 50) - 1); };
    const unsigned xh0 = c * 128 + ((g ^ (c >> 1)) & 7) * 16, xh1 = xh0 ^ 64;
    u64 tag = 0;
#define GET_TAG() do { tag = mix64(*(const __attribute__((address_space(4))) u64*)(ws + WS_EPOCH)); asm volatile("" : "+s"(tag)); } while (0)
    u2 o[5];
    const __amdgpu_buffer_rsrc_t rs = __builtin_amdgcn_make_buffer_rsrc(ws, 0, WS_TOTAL, 0x00020000);
    const unsigned obase = WS_O + (((br * 16 + rt) * 4 + cw) * 8 + hg) * 5120;

    if (br == 0) {
        if (par) {
            const int r1 = lane >= 30;
            const unsigned voffw = ((cw >> 1) * 8 + (cw & 1) * 2 + r1) * 3840 + (r10 - 3 * r1) * 1280 + l10;
            const bool halo = cw < 2;
            const int hdelta = rt ? (-15 - 17 * cw) * 1280 : 0;
            auto ldw = [&](int t, unsigned sl, int i0, int i1) {
                const int ks = (int)(seq >> (4 * t)) & 15;
                const char* wb = w + (size_t)ks * (64 * 3840);
                if (i0 == 0 && halo) glds16(x + ks * 128 + hdelta, voffx, sl + cw * 1024);
#pragma unroll
                for (int i = i0; i < i1; ++i) glds16m(wb + ((i >> 1) * 16 + (i & 1) * 4) * 3840, voffw, sl + XB + cw * 960 + i * 3840, M60);
            };
            ldw(0, 2 * STB, 0, 8);
            ldx(1, 2048);
            wait_vm<4>();
            bar();
            ldw(1, 0, 0, NH);
            unsigned s1 = 0, s2 = STB;
#pragma unroll 1
            for (int t = 0; t < 8; ++t) {
                ldw(t + 1, s1, NH, 8); ldx(t + 2, s2 + 2048); ldw(t + 2, s2, 0, NH);
                s1 = s2; s2 += STB; if (s2 == 3 * STB) s2 = 0;
                if (halo) wait_vm<5 + NH>(); else wait_vm<4 + NH>();
                bar();
            }
            ldw(9, 2 * STB, NH, 8);
            wo_touch(DUMMY_A + cw * 256);
            wo_load(IC<0>{}, IC<WOA0>{}, IC<1>{}, 0);
            wait_vm<1 + WOA0>();
            bar();
            wo_load(IC<WOA0>{}, IC<8 - WOA0>{}, IC<1>{}, 0);
            wo_load(IC<12>{}, IC<4>{}, IC<1>{}, 0);
            wait_vm<0>();
            __builtin_amdgcn_s_setprio(0);
        } else {
            GET_TAG();
            const unsigned wlane = XB + (16 * (g >> 1) + 4 * (g & 1) + ((lane >> 2) & 3)) * 480 + (lane & 3) * 8;
            auto mainloop = [&](auto MC) {
                constexpr int MODE = decltype(MC)::value;
                constexpr int NT = a_nt(MODE), WM = MODE >> 1;
                constexpr unsigned XO = (1 + 4 * WM) * 2048;
                f4 acc[8][4], ha[4];
#pragma unroll
                for (int n = 0; n < 8; ++n)
#pragma unroll
                    for (int m = 0; m < 4; ++m) if (n < NT && a_main(MODE, a_tile(MODE, n))) { acc[n][m] = z4; asm volatile("" : "+v"(acc[n][m])); }
#pragma unroll
                for (int n = 0; n < 4; ++n) ha[n] = z4;
                bf8 xk[2][5];
                auto rdh = [&](unsigned wa, int ts, int h) { return ldtr(wa + (ts / NT) * 15360 + a_tile(MODE, ts % NT) * 32 + h * 3840); };
                auto rdx = [&](unsigned xa, int m) { return lds128(xa + (m < 4 ? XO + m * 2048 : 0)); };
                constexpr SPlan SP = a_plan(MODE);
                const unsigned lw_ = opaque(wlane), lx0 = opaque(xh0), lx1 = opaque(xh1);
                unsigned wa = opaque(2 * STB + lw_), xa0 = opaque(2 * STB + lx0), xa1 = opaque(2 * STB + lx1);
                unsigned slot = 2 * STB;
                bf4 lo[2 * NT], hi[2 * NT];
                auto doread = [&](auto AC) {
                    constexpr int a = decltype(AC)::value;
                    if constexpr (a >= 64) xk[(a - 64) >> 3][a & 7] = rdx(((a - 64) >> 3) ? xa1 : xa0, a & 7);
                    else if constexpr (a >= 0) { if constexpr (a & 1) hi[a >> 1] = rdh(wa, a >> 1, 1); else lo[a >> 1] = rdh(wa, a >> 1, 0); }
                };
                auto domfma = [&](auto TSC, auto MC) {
                    constexpr int ts = decltype(TSC)::value, m = decltype(MC)::value, kk = ts / NT, i = ts % NT, n = a_tile(MODE, i);
                    constexpr bool Y = n < 10;
                    const bf8 wf = cat(lo[ts], hi[ts]);
                    if constexpr (m < 4) acc[i][m] = Y ? mfma(wf, xk[kk][m], acc[i][m]) : mfma(xk[kk][m], wf, acc[i][m]);
                    else { constexpr int h = a_hidx(MODE, n); ha[h] = Y ? mfma(wf, xk[kk][4], ha[h]) : mfma(xk[kk][4], wf, ha[h]); }
                };
                wait_vm<0>();
                bar();
                static_for<SP.nwrap>([&](auto KC) { doread(IC<SP.wrap[decltype(KC)::value]>{}); SB; });
#pragma unroll 1
                for (int t = 0; t < 9; ++t) {
                    static_for<SP.ns>([&](auto QC) {
                        constexpr int q = decltype(QC)::value;
                        if constexpr (q == SP.bpos) { bar(); SB; }
                        domfma(IC<SP.ts[q]>{}, IC<SP.m[q]>{});
                        doread(IC<SP.at[q]>{});
                        if constexpr (q == SP.bpos - 6) { slot += STB; asm volatile("" : "+s"(slot)); }
                        if constexpr (q == SP.bpos - 5) { slot = slot == 3 * STB ? 0 : slot; asm volatile("" : "+s"(slot)); }
                        if constexpr (q == SP.bpos - 3) wa = opaque(slot + lw_);
                        if constexpr (q == SP.bpos - 2) xa0 = opaque(slot + lx0);
                        if constexpr (q == SP.bpos - 1) xa1 = opaque(slot + lx1);
                        SB;
                    });
                }
                {
                    constexpr ALPlan PL = al_plan(MODE);
                    const unsigned ew = opaque(EX_OFF + lane * 8), eq = opaque(EXQ_OFF + lane * 8);
                    auto put = [&](auto PC) {
                        constexpr int p = decltype(PC)::value, i = p / 5, m = p % 5, n = a_tile(MODE, i);
                        if constexpr (m < 4) sts64(n < 5 ? eq + ((4 * WM + m) * 5 + n) * 512 : ew + ((2 + 4 * WM + m) * 10 + n - 5) * 512, pk4(acc[i][m]));
                        else {
                            const u2 v = pk4(ha[a_hidx(MODE, n)]);
                            sts64(ew + (n - 5) * 512, v);
                            sts64((ew ^ (n < 10 ? 64 : 256)) + (10 + n - 5) * 512, v);
                        }
                    };
                    auto gap = [&](auto QC) {
                        constexpr int a = PL.act[decltype(QC)::value];
                        if constexpr (a >= 100) put(IC<a - 100>{}); else doread(IC<a>{});
                        SB;
                    };
                    static_for<2 * NT>([&](auto UC) {
                        constexpr int u = decltype(UC)::value, i = al_tile(MODE, u), kk = al_kk(MODE, u), n = a_tile(MODE, i);
                        constexpr int q0 = al_start(MODE, u);
                        if constexpr (a_main(MODE, n)) static_for<4>([&](auto MC) { domfma(IC<kk * NT + i>{}, MC); gap(IC<q0 + decltype(MC)::value>{}); });
                        if constexpr (a_halo(MODE, n)) { domfma(IC<kk * NT + i>{}, IC<4>{}); gap(IC<q0 + (a_main(MODE, n) ? 4 : 0)>{}); }
                    });
                    static_for<5 * NT - PL.rest>([&](auto PC) { constexpr int p = PL.rest + decltype(PC)::value; if constexpr (al_has(MODE, p)) put(IC<p>{}); });
                }
            };
            if (cw == 0) mainloop(IC<0>{});
            else if (cw == 1) mainloop(IC<1>{});
            else if (cw == 2) mainloop(IC<2>{});
            else mainloop(IC<3>{});
        }
        const AttnMask mk = attn_mask((rt | cw) != 0, lane);
        const unsigned ep = opaque(EX_OFF + (2 * cw + par) * 5120 + lane * 8);
        const unsigned eq = opaque(EXQ_OFF + (2 * cw + par) * 2560 + lane * 8);
        bar();
        u2 q[5];
        bf8 kfo[4], kfp[4], vf[5];
#pragma unroll
        for (int i = 0; i < 5; ++i) q[i] = lds64(eq + i * 512);
#pragma unroll
        for (int h = 0; h < 4; ++h) { kfo[h] = lds2x64(ep, 20 + h, 21 + h); kfp[h] = lds2x64(ep, h, h + 1); }
#pragma unroll
        for (int i = 0; i < 5; ++i) vf[i] = lds2x64(ep, 5 + i, 25 + i);
        wo_load(IC<8>{}, IC<2>{}, IC<2>{}, par);
        asm volatile("s_waitcnt lgkmcnt(0)" : "+v"(kfo[0]), "+v"(kfo[1]), "+v"(kfo[2]), "+v"(kfo[3]), "+v"(kfp[0]), "+v"(kfp[1]), "+v"(kfp[2]), "+v"(kfp[3]),
                     "+v"(vf[0]), "+v"(vf[1]), "+v"(vf[2]), "+v"(vf[3]), "+v"(vf[4]));
        attn_tile(q, kfo, kfp, vf, mk, lane, o);
        store_o(rs, obase, par, lane, o);
    } else {
        u2 q[5];
        const unsigned pq = opaque(PQ_OFF + cw * 2560 + lane * 8);
        if (par) {
            unsigned voffw[6];
            const int r22 = (lane * 47) >> 10, c22 = min(lane - 22 * r22, 19);
            const unsigned lkv = 1280 + (c22 >= 10 ? 1120 : 0) + c22 * 16 + hg * 160 + cw * 16 * 3840;
            auto ldw = [&](int t, int i0, int i1) {
                const char* wb = w + (size_t)((int)(seq >> (4 * t)) & 15) * (64 * 3840);
                const unsigned dst = RB0 + ((t + 2) % 3) * STBB + XBB + cw * (KVB / 4);
#pragma unroll
                for (int i = i0; i < i1; ++i) {
                    if (i < 5) glds16(wb, voffw[i], dst + i * 1056); else glds16m(wb, voffw[i], dst + i * 1056, (1ull << 20) - 1);
                }
            };
#pragma unroll
            for (int i = 0; i < 6; ++i) {
                const int pos = min(3 * i + r22, 15);
                voffw[i] = ((pos & 3) | ((pos & 4) << 1) | ((pos & 8) >> 1)) * 3840 + lkv;
                ldw(0, i, i + 1);
                SB;
            }
            ldx(1, RB0);
            SB;
            unsigned xad0[3], xad1[3];
#pragma unroll
            for (int i = 0; i < 3; i += 2) { xad0[i] = opaque(RB0 + i * STBB + cw * 4096 + xh0); xad1[i] = opaque(RB0 + i * STBB + cw * 4096 + xh1); }
            xad0[1] = xad0[0] + STBB; xad1[1] = xad1[0] + STBB;
            unsigned voffq[3];
            auto wq_unit = [&](int T) {
                const char* wb = w + (size_t)((int)(seq >> (4 * T)) & 15) * (64 * 3840);
#pragma unroll
                for (int i = 0; i < 3; ++i) glds16m(wb, voffq[i], WQ_OFF + T * 10240 + cw * 2560 + i * 960, i < 2 ? M60 : (1ull << 40) - 1);
            };
            bf8 xf[20][2];
            wait_vm<4>();
            bar();
            static_for<10>([&](auto TC) {
                constexpr int t = decltype(TC)::value, sl = (t + 2) % 3;
                if constexpr (t == 0) ldw(1, 0, NHB);
                if constexpr (t < 9) ldw(t + 1, NHB, 6);
                xf[2 * t][0] = lds128(xad0[sl]); xf[2 * t][1] = lds128(xad0[sl] + 2048);
                xf[2 * t + 1][0] = lds128(xad1[sl]); xf[2 * t + 1][1] = lds128(xad1[sl] + 2048);
                if constexpr (t < 8) { ldx(t + 2, RB0 + ((t + 4) % 3) * STBB); ldw(t + 2, 0, NHB); }
                if constexpr (t == 7) {
#pragma unroll
                    for (int i = 0; i < 3; ++i) {
                        const int pos = min(6 * i + r10, 15);
                        voffq[i] = opaque((cw * 16 + ((pos & 3) | ((pos & 4) << 1) | ((pos & 8) >> 1))) * 3840 + l10);
                    }
                }
                if constexpr (t < 8) wait_vm<4 + NHB>();
                else if constexpr (t == 8) { static_for<QB>([&](auto UC) { wq_unit(decltype(UC)::value); }); wait_vm<3 * QB>(); }
                if constexpr (t < 9) bar();
            });
            {
                const u4 zz = {0u, 0u, 0u, 0u};
#pragma unroll
                for (int i = 0; i < 3; ++i) if (i < 2 || tid - 256 < 112) sts128(IMG_OFF + i * 4096 + (tid - 256) * 16, zz);
            }
            cnt_add(CNT_OFF + 16);
            wq_unit(QB);
            __builtin_amdgcn_s_setprio(0);
#pragma unroll 1
            for (int T = QB + 1; T < QP; ++T) wq_unit(T);
            wait_vm<3 * (QP - 1 - QU)>();
            bar();
            f4 aq[2][5];
#pragma unroll
            for (int n = 0; n < 5; ++n) { aq[0][n] = one4; aq[1][n] = one4; }
            const unsigned ql0 = opaque(WQ_OFF + (16 * (g >> 1) + 4 * (g & 1) + ((lane >> 2) & 3)) * 160 + (lane & 3) * 8), ql1 = opaque(ql0 + 51200);
            bf4 lo[21][5], hi[21][5];
#pragma unroll
            for (int i = 0; i < 5; ++i) { lo[0][i] = ldtr(ql0 + i * 32); hi[0][i] = ldtr(ql0 + i * 32 + 1280); SB; }
            static_for<20>([&](auto SC) {
                constexpr int s = decltype(SC)::value;
                if constexpr (s + QP == 9) wo_touch(WQ_OFF + 9 * 10240 + cw * 2560);
                if constexpr (s + QP < 10) wq_unit(s + QP);
                if constexpr (s == 2 * QU + 1) { wait_vm<0>(); cnt_add(CNT_OFF + 8); cnt_wait(CNT_OFF + 8, 4); }
                SB;
                const unsigned na = (s + 1 < 10 ? ql0 : ql1) + ((s + 1) % 10) * 5120;
#pragma unroll
                for (int i = 0; i < 5; ++i) {
                    const bf8 wf = cat(lo[s][i], hi[s][i]);
                    aq[0][i] = mfma(wf, xf[s][0], aq[0][i]); if (s < 19) lo[s + 1][i] = ldtr(na + i * 32); SB;
                    aq[1][i] = mfma(wf, xf[s][1], aq[1][i]); if (s < 19) hi[s + 1][i] = ldtr(na + i * 32 + 1280); SB;
                }
            });
#pragma unroll
            for (int i = 0; i < 5; ++i) sts64(pq + i * 512, pk4(phi4(aq[0][i])));
#pragma unroll
            for (int i = 0; i < 5; ++i) q[i] = pk4(phi4(aq[1][i]));
        } else {
            GET_TAG();
            if (tid < 8) *(LAS unsigned*)(size_t)(CNT_OFF + tid * 4) = 0;
            f4 acc[6][4];
#pragma unroll
            for (int n = 0; n < 6; ++n)
#pragma unroll
                for (int m = 0; m < 4; ++m) if (m < 2 || n % 3 != 2) { acc[n][m] = n < 3 ? one4 : z4; asm volatile("" : "+v"(acc[n][m])); }
            const unsigned pbase = WS_KVP + (hg * 16 + rt) * KVP_STRIDE;
            unsigned off[5];
            {
                const int Q = 4 * cw + g;
#pragma unroll
                for (int a = 0; a < 3; ++a) {
                    const int et = cw - 1 + a, e = 16 * et + c, h = (e * 205) >> 12, u = Q - 5 * h;
                    off[a] = opaque((et >= 0 && (unsigned)u <= 4u) ? pbase + (u * 80 + e) * 8 : 0xF0000000u);
                }
                off[3] = opaque(c == 0 ? pbase + (400 + Q) * 8 : 0xF0000000u);
                const int e = 16 * (cw + 1) + c;
                off[4] = opaque(cw >= 2 ? (e >= 60 ? pbase + ((1 + g) * 80 + e) * 8 : 0xF0000000u) : ((cw == 1 && c == 0) ? pbase + (416 + g) * 8 : 0xF0000000u));
            }
            const unsigned a1 = opaque(EXB_OFF + cw * 1024 + lane * 16), a0 = opaque(EXB_OFF + max(cw - 1, 0) * 1024 + lane * 16), a4 = opaque(EXB_OFF + lane * 16);
            unsigned pvo[4], isl[4];
#pragma unroll
            for (int r = 0; r < 4; ++r) pvo[r] = opaque(WS_KVP + (hg * 16 + ((4 * r + g + 2 * rt) & 15)) * KVP_STRIDE + cw * 256 + c * 16);
#pragma unroll
            for (int i = 0; i < 4; ++i) {
                const int id = 2 * ((cw + 4 * i) * 16 + c) + (g & 1);
                const int u = (id * 205) >> 14, e = id - u * 80, h = (e * 205) >> 12, Q = 5 * h + u;
                const int ti = e >> 4, pr = Q >> 3;
                const int f = 2 * ti + pr - (ti > 2 ? 2 : (ti > 0 ? 1 : 0));
                const unsigned kvs = f * 1024 + ((Q & 3) * 16 + (e & 15)) * 16 + ((Q >> 2) & 1) * 8;
                const unsigned zs = 9216 + (e >> 3) * 256 + ((e & 3) * 4 + ((e * 205) >> 10)) * 16 + ((e >> 2) & 1) * 8;
                isl[i] = opaque((g < 2 && id < 420) ? IMG_OFF + (id < 400 ? kvs : zs) : CNT_OFF + 128);
            }
            u4 sel4;
#pragma unroll
            for (int d = 0; d < 4; ++d) sel4[d] = c == 2 * d ? 0x3F80u : (c == 2 * d + 1 ? 0x3F800000u : 0u);
            asm volatile("" : "+v"(sel4));
            const bf8 sel = __builtin_bit_cast(bf8, sel4);
            const unsigned exo = opaque(EXB_OFF + cw * 10240 + (cw & 1) * 3072 + lane * 16), exp_ = opaque(EXB_OFF + (cw ^ 1) * 10240 + (cw & 1) * 3072 + lane * 16),
                           ex2 = opaque(EXB_OFF + cw * 10240 + 2048 + lane * 16);
            const unsigned wl_ = XBB + (16 * (g >> 1) + 4 * (g & 1) + ((lane >> 2) & 3)) * 352 + (lane & 3) * 8;
            unsigned lp[6] = {cw * 4096 + xh0, (cw ^ 1) * 4096 + xh0, cw * 4096 + xh1, (cw ^ 1) * 4096 + xh1, wl_ + (cw & 1) * 96, wl_ + 64};
            unsigned ad[6];
#pragma unroll
            for (int i = 0; i < 6; ++i) { lp[i] = opaque(lp[i]); ad[i] = opaque(lp[i] + RB0 + 2 * STBB); }
            unsigned slot = RB0 + 2 * STBB;
            bf8 xk[2][4];
            bf4 lo[18], hi[18];
            auto doread = [&](auto AC) {
                constexpr int a = decltype(AC)::value;
                if constexpr (a >= 64) { constexpr int kk = (a - 64) >> 3, m = a & 7; xk[kk][m] = lds128(ad[2 * kk + (m >> 1)] + (m & 1) * 2048); }
                else if constexpr (a >= 0) {
                    constexpr int f = a >> 1, h = a & 1, n = f % 9, kk = f / 9;
                    const bf4 v = ldtr((n % 3 == 2 ? ad[5] : ad[4] + (n % 3) * 32) + (n / 3) * 160 + kk * (KVB / 2) + h * 2816);
                    if constexpr (h) hi[f] = v; else lo[f] = v;
                }
            };
            auto domfma = [&](auto FC, auto MC) {
                constexpr int f = decltype(FC)::value, m = decltype(MC)::value, n = f % 9, kk = f / 9;
                acc[n][m] = mfma(xk[kk][m], cat(lo[f], hi[f]), acc[n][m]);
            };
            constexpr SPlan SP = b_plan(BACKK);
            wait_vm<0>();
            bar();
            static_for<SP.nwrap>([&](auto KC) { doread(IC<SP.wrap[decltype(KC)::value]>{}); SB; });
#pragma unroll 1
            for (int t = 0; t < 9; ++t) {
                static_for<SP.ns>([&](auto QC) {
                    constexpr int q_ = decltype(QC)::value;
                    if constexpr (q_ == SP.bpos) { bar(); SB; }
                    domfma(IC<SP.ts[q_]>{}, IC<SP.m[q_]>{});
                    doread(IC<SP.at[q_]>{});
                    if constexpr (q_ == 1) { slot += STBB; asm volatile("" : "+s"(slot)); }
                    if constexpr (q_ == 3) { slot = slot == RB0 + 3 * STBB ? RB0 : slot; asm volatile("" : "+s"(slot)); }
                    if constexpr (q_ == SP.bpos - 4) { ad[0] = opaque(lp[0] + slot); ad[1] = opaque(lp[1] + slot); }
                    if constexpr (q_ == SP.bpos - 3) { ad[2] = opaque(lp[2] + slot); ad[3] = opaque(lp[3] + slot); }
                    if constexpr (q_ == SP.bpos - 2) ad[4] = opaque(lp[4] + slot);
                    if constexpr (q_ == SP.bpos - 1) ad[5] = opaque(lp[5] + slot);
                    SB;
                });
            }
            __builtin_amdgcn_s_setprio(2);
            static_for<BLF>([&](auto PC) {
                constexpr int p = decltype(PC)::value;
                static_for<b_nm(p)>([&](auto MC) { domfma(IC<bl_f(p)>{}, MC); doread(IC<bl_act(b_cum(p) + decltype(MC)::value)>{}); SB; });
            });
            cnt_add(CNT_OFF + 16);
            SB;
            auto pack = [&](int n, int mp) {
                const u2 a = pk4(n < 3 ? phi4(acc[n][2 * mp]) : acc[n][2 * mp]), b = pk4(n < 3 ? phi4(acc[n][2 * mp + 1]) : acc[n][2 * mp + 1]);
                const u4 t = {a.x, a.y, b.x, b.y};
                return t;
            };
            auto dst = [&](int n, int mp) { return (n % 3 == 2 ? ex2 : (mp ? exp_ : exo) + (n % 3) * 1024) + (n / 3) * 5120; };
            static_for<10 - BLF>([&](auto PC) { constexpr int p = BLF + decltype(PC)::value; static_for<b_nm(p)>([&](auto MC) { domfma(IC<bl_f(p)>{}, MC); }); });
            u4 f0 = pack(0, 0), f1 = pack(0, 1);
#pragma unroll
            for (int i = 0; i < b_cum(10) - b_cum(BLF); ++i) { __builtin_amdgcn_sched_group_barrier(0x8, 1, 0); __builtin_amdgcn_sched_group_barrier(0x402, 5, 0); }
            asm volatile("" : "+v"(f0), "+v"(f1));
            cnt_wait(CNT_OFF + 16, 8);
            sts128(dst(0, 0), f0); sts128(dst(0, 1), f1);
            static_for<2>([&](auto PC) { constexpr int p = 10 + decltype(PC)::value; static_for<b_nm(p)>([&](auto MC) { domfma(IC<bl_f(p)>{}, MC); }); });
            sts128(dst(3, 0), pack(3, 0)); sts128(dst(3, 1), pack(3, 1)); sts128(dst(1, 0), pack(1, 0));
#pragma unroll
            for (int i = 0; i < 6; ++i) { __builtin_amdgcn_sched_group_barrier(0x8, 1, 1); __builtin_amdgcn_sched_group_barrier(0x402, 5, 1); }
            SB;
            sts128(dst(4, 0), pack(4, 0)); sts128(dst(4, 1), pack(4, 1)); sts128(dst(5, 0), pack(5, 0));
            SB;
            sts128(dst(1, 1), pack(1, 1));
            SB;
            sts128(dst(2, 0), pack(2, 0));
            bar();
            {
                const u4 ones4 = {0x3F803F80u, 0x3F803F80u, 0x3F803F80u, 0x3F803F80u};
                const bf8 ones = __builtin_bit_cast(bf8, ones4);
                f4 t[3] = {z4, z4, z4}, tz = z4, t4 = z4;
                bf8 kf[4], v0[4], v1[4], v2[4], k4[4];
#pragma unroll
                for (int s = 0; s < 4; ++s) {
                    kf[s] = lds128(a1 + s * 10240);
                    v0[s] = lds128(a0 + s * 10240 + 5120);
                    v1[s] = lds128(a1 + s * 10240 + 5120);
                    v2[s] = lds128(a1 + s * 10240 + 6144);
                    k4[s] = lds128(a4 + s * 10240 + 4096);
                    SB;
                }
#pragma unroll
                for (int s = 0; s < 4; ++s) {
                    t[0] = mfma(kf[s], v0[s], t[0]); t[1] = mfma(kf[s], v1[s], t[1]); t[2] = mfma(kf[s], v2[s], t[2]); tz = mfma(kf[s], ones, tz);
                }
                if (cw < 2) {
#pragma unroll
                    for (int s = 0; s < 4; ++s) t4 = mfma(k4[s], ones, t4);
                } else {
#pragma unroll
                    for (int s = 0; s < 4; ++s) t4 = mfma(k4[s], v2[s], t4);
                }
#pragma unroll
                for (int a = 0; a < 3; ++a) __builtin_amdgcn_raw_buffer_store_b64(pk4(t[a]), rs, off[a], 0, 16);
                __builtin_amdgcn_raw_buffer_store_b64(pk4(tz), rs, off[3], 0, 16);
                __builtin_amdgcn_raw_buffer_store_b64(pk4(t4), rs, off[4], 0, 16);
                asm volatile("" ::: "memory");
                if (lane == 0) __hip_atomic_fetch_add((LAS unsigned*)(size_t)(CNT_OFF + 4), 1u, __ATOMIC_RELAXED, __HIP_MEMORY_SCOPE_WORKGROUP);
            }
            SB;
            wait_vm<0>();
            {
                unsigned old = 0;
                if (lane == 0) old = __hip_atomic_fetch_add((LAS unsigned*)(size_t)CNT_OFF, 1u, __ATOMIC_RELAXED, __HIP_MEMORY_SCOPE_WORKGROUP);
                if (lane == 0 && old == 3) st_agent(ws + WS_FKV + (hg * 16 + rt) * 128, tag);
            }
            cnt_wait(CNT_OFF + 4, 4);
            wo_load(IC<0>{}, IC<WOB1>{}, IC<1>{}, 0);
            poll_all(ws + WS_FKV + (hg * 16 + c) * 128, tag);
            u4 pa[4][4];
            const unsigned oob = (cw == 1 && c >= 2) ? 0xF0000000u : 0u;
#pragma unroll
            for (int r = 0; r < 4; ++r)
#pragma unroll
                for (int i = 0; i < 4; ++i) if (i < 3 || cw < 2) pa[i][r] = __builtin_amdgcn_raw_buffer_load_b128(rs, (i < 3 ? pvo[r] : pvo[r] | oob) + i * 1024, 0, 16);
#pragma unroll
            for (int i = 0; i < 4; ++i)
                if (i < 3 || cw < 2) {
                    f4 sm = z4;
#pragma unroll
                    for (int r = 0; r < 4; ++r) sm = mfma(sel, B8(pa[i][r]), sm);
                    sts64(isl[i], pk4(sm));
                }
            wo_load(IC<WOB1>{}, IC<16 - WOB1>{}, IC<1>{}, 0);
            __builtin_amdgcn_s_setprio(0);
        }
        const unsigned ia = opaque(IMG_OFF + lane * 16);
        const unsigned za = opaque(IMG_OFF + 9216 + (g * 4 + (c & 3)) * 16);
        bar();
        if (!par) {
#pragma unroll
            for (int i = 0; i < 5; ++i) q[i] = lds64(pq + i * 512);
        }
        const bf8 p01 = cat(q[0], q[1]), p23 = cat(q[2], q[3]);
        const u2 q4 = q[4];
        const bf8 zf0 = lds128(za), zf1 = lds128(za + 256), zf2 = lds128(za + 512);
        bf8 kf[9];
#pragma unroll
        for (int i = 0; i < 9; ++i) kf[i] = lds128(ia + i * 1024);
        SB;
        const u2 zero2 = {0u, 0u};
        const bf8 p4 = cat(q4, zero2);
        f4 den = mfma(zf2, p4, mfma(zf1, p23, mfma(zf0, p01, z4)));
        f4 n0 = mfma(kf[0], p01, z4);
        f4 n1 = mfma(kf[2], p23, mfma(kf[1], p01, z4));
        f4 n2 = mfma(kf[4], p23, mfma(kf[3], p01, z4));
        f4 n3 = mfma(kf[6], p4, mfma(kf[5], p23, z4));
        f4 n4 = mfma(kf[8], p4, mfma(kf[7], p23, z4));
        float r0 = __builtin_amdgcn_rcpf(den[0] + 1e-6f), r1_ = __builtin_amdgcn_rcpf(den[1] + 1e-6f);
        float r2 = __builtin_amdgcn_rcpf(den[2] + 1e-6f), r3 = __builtin_amdgcn_rcpf(den[3] + 1e-6f);
        o[0] = pk4(n0 * r0);
        o[1] = pk4(n1 * (g < 1 ? r0 : r1_));
        o[2] = pk4(n2 * (g < 2 ? r1_ : r2));
        o[3] = pk4(n3 * (g < 3 ? r2 : r3));
        o[4] = pk4(n4 * r3);
        store_o(rs, obase, par, lane, o);
    }
    {
        unsigned wl[4];
#pragma unroll
        for (int u = 0; u < 4; ++u) wl[u] = opaque((lane >> 2) * 160 + (lane & 3) * 8 + (br ? wo_pair(1, u) : wo_pair(0, u)));
        auto rdw = [&](int s, int i) {
            const int u = s / 5, r = s % 5;
            const unsigned b = wl[u] + i * 32;
            return r < 4 ? cat(ldtr(b + ((r >> 1) * 80 + (r & 1) * 32) * 160), ldtr(b + ((r >> 1) * 80 + (r & 1) * 32 + 16) * 160)) : cat(ldtr(b + 64 * 160), ldtr(b + 144 * 160));
        };
        bf8 wf[20][5];
        SB;
#pragma unroll
        for (int s = 0; s < PRE2A; ++s)
#pragma unroll
            for (int i = 0; i < 5; ++i) wf[s][i] = rdw(s, i);
        SB;
        wait_vm<0>();
        bar();
        if (tid == 0) st_agent(ws + WS_FO + ((br * 16 + rt) * 8 + hg) * 128, tag);
        wo_load(IC<16>{}, IC<8>{}, IC<2>{}, par);
#pragma unroll
        for (int s = PRE2A; s < PRE2; ++s)
#pragma unroll
            for (int i = 0; i < 5; ++i) wf[s][i] = rdw(s, i);
        unsigned so[8];
        const unsigned ob = WS_O + ((br * 16 + rt2) * 4 + cw) * 8 * 5120;
#pragma unroll
        for (int gg = 0; gg < 8; ++gg) so[gg] = ob + ((gg + ct) & 7) * 5120;
        f4 a2[5];
#pragma unroll
        for (int n = 0; n < 5; ++n) { a2[n] = z4; asm volatile("" : "+v"(a2[n])); }
        SB;
        if (wv == 0) poll_all(ws + WS_FO + ((br * 16 + rt2) * 8 + (lane & 7)) * 128, tag);
        wait_vm<0>();
        bar();
        u4 of[8][2];
        u2 ot[8];
        const unsigned v16 = par * 2048 + lane * 16, v8 = 4096 + par * 512 + lane * 8;
        auto ld = [&](auto SC) {
            constexpr int s = decltype(SC)::value;
            if constexpr (s < 20) {
                constexpr int u = s / 5, r = s % 5;
                if constexpr (r < 4) of[2 * u + (r >> 1)][r & 1] = __builtin_amdgcn_raw_buffer_load_b128(rs, v16 + (r & 1) * 1024, so[2 * u + (r >> 1)], 16);
                else {
                    ot[2 * u] = __builtin_amdgcn_raw_buffer_load_b64(rs, v8, so[2 * u], 16);
                    ot[2 * u + 1] = __builtin_amdgcn_raw_buffer_load_b64(rs, v8, so[2 * u + 1], 16);
                }
            }
        };
        static_for<P2AHEAD>([&](auto SC) { ld(SC); });
        SB;
        const bool bump = (bid == 0) & (wv == 1);
        u64 ef[4] = {0, 0, 0, 0};
        constexpr int NREM = (20 - PRE2) * 5, PER = (NREM + 17) / 18;
        static_for<20>([&](auto SC) {
            constexpr int s = decltype(SC)::value;
            constexpr int u = s / 5, r = s % 5;
            if constexpr (s == 15) {
                if (bump) {
#pragma unroll
                    for (int i = 0; i < 4; ++i) ef[i] = ld_agent(ws + WS_FO + (i * 64 + lane) * 128);
                }
            }
            bf8 o0;
            if constexpr (r < 4) o0 = B8(of[2 * u + (r >> 1)][r & 1]); else o0 = cat(ot[2 * u], ot[2 * u + 1]);
#pragma unroll
            for (int i = 0; i < 5; ++i) {
                a2[i] = mfma(wf[s][i], o0, a2[i]);
                const int fi = s * PER + i - 1;
                if (i >= 1 && i - 1 < PER && fi < NREM) wf[PRE2 + fi / 5][fi % 5] = rdw(PRE2 + fi / 5, fi % 5);
                if (i == 0) ld(IC<s + P2AHEAD>{});
                SB;
            }
        });
        bool bumped = !bump;
        if (bump && __all((ef[0] == tag) & (ef[1] == tag) & (ef[2] == tag) & (ef[3] == tag))) { if (lane == 0) st_agent(ws + WS_EPOCH, tag); bumped = true; }
        const unsigned tb = (br ? OT_B : OT_A) + wv * 2816;
#pragma unroll
        for (int n = 0; n < 5; ++n) sts64(tb + c * 176 + n * 32 + g * 8, pk4(a2[n]));
        asm volatile("" ::: "memory");
        const __amdgpu_buffer_rsrc_t ro = __builtin_amdgcn_make_buffer_rsrc(out, 0, 2048 * 2560, 0x00020000);
        const unsigned opb = (rt2 * 128 + 32 * cw + par) * 2560 + br * 1280 + ct * 160;
#pragma unroll
        for (int i = 0; i < 3; ++i) {
            const int id = lane + 64 * i, row = (id * 205) >> 11, ch = id - row * 10;
            __builtin_amdgcn_raw_buffer_store_b128(*(LAS u4*)(size_t)(tb + row * 176 + ch * 16), ro, (i < 2 || lane < 32) ? row * 5120 + ch * 16 : 0xF0000000u, opb, 0);
        }
        asm volatile("fk_tail_label:");
        if (!bumped) {
            const char* p = ws + WS_FO + lane * 128;
#pragma unroll 1
            for (unsigned spins = 0; spins < (1u << 18); ++spins) {
                if (__all((ef[0] == tag) & (ef[1] == tag) & (ef[2] == tag) & (ef[3] == tag))) break;
                __builtin_amdgcn_s_sleep(2);
#pragma unroll
                for (int i = 0; i < 4; ++i) ef[i] = ld_agent(p + i * 8192);
            }
            if (lane == 0) st_agent(ws + WS_EPOCH, tag);
        }
    }
}

extern "C" void kernel_launch(void* const* d_in, const int* in_sizes, int n_in, void* d_out, int out_size, void* d_ws, size_t ws_size,
                              hipStream_t stream) {
    const char *xA = (const char*)d_in[0], *wA = (const char*)d_in[1], *woA = (const char*)d_in[2];
    const char *xB = (const char*)d_in[3], *wB = (const char*)d_in[4], *woB = (const char*)d_in[5];
    (void)hipFuncSetAttribute((const void*)fused_kernel, hipFuncAttributeMaxDynamicSharedMemorySize, LDS_TOTAL);
    fused_kernel<<<256, 512, LDS_TOTAL, stream>>>(xA, wA, xB, wB, woA, woB, (char*)d_out, (char*)d_ws);
}
```
